```python
import torch

def _softmax(x, axis=-1):
    return torch.softmax(x, dim=axis)

def _gelu(x):
    return torch.nn.functional.gelu(x, approximate='tanh')

def _silu(x):
    return torch.nn.functional.silu(x)

def _sigmoid(x):
    return torch.sigmoid(x)

def _layernorm(x, axis=-1, eps=1e-05):
    m = x.mean(dim=axis, keepdim=True)
    v = x.var(dim=axis, keepdim=True, unbiased=False)
    return (x - m) / torch.sqrt(v + eps)

def _scatter_add(tgt, idx, src, axis=0):
    return tgt.clone().index_add_(axis, idx, src)

def _segment_sum(src, seg, n):
    out = torch.zeros((n,) + src.shape[1:], dtype=src.dtype)
    return out.index_add_(0, seg, src)

def _pad(x, pw):
    flat = tuple((v for pair in reversed(pw) for v in pair))
    return torch.nn.functional.pad(x, flat)
kernel_name = 'gated_mix__spectral_fnet__mixer_gmlp__prompt'

def setup_inputs():

    _w = lambda a: (lambda t: t.to(torch.bfloat16) if t.is_floating_point() else t)(torch.as_tensor(a))

    def _A_setup():
        B, S, D = (1, 2048, 448)
        ns = torch.arange(S)[:, None] * torch.arange(S)[None, :]
        nd = torch.arange(D)[:, None] * torch.arange(D)[None, :]
        Ws_re = torch.cos(-2.0 * torch.pi * ns / S).to(float)
        Ws_im = torch.sin(-2.0 * torch.pi * ns / S).to(float)
        Wd_re = torch.cos(-2.0 * torch.pi * nd / D).to(float)
        Wd_im = torch.sin(-2.0 * torch.pi * nd / D).to(float)
        return {'x': _w((lambda *s: torch.randn(*s, dtype=torch.bfloat16) * 0.05)(B, S, D)), 'dft_seq_re': _w(Ws_re), 'dft_seq_im': _w(Ws_im), 'dft_hid_re': _w(Wd_re), 'dft_hid_im': _w(Wd_im)}
    _A_in = _A_setup()

    def _B_setup():
        B, S, D, F = (1, 2048, 448, 1792)
        Fh = F // 2
        return {'x': _w((lambda *s: torch.randn(*s, dtype=torch.bfloat16) * 0.05)(B, S, D)), 'w_in': _w((lambda *s: torch.randn(*s, dtype=torch.bfloat16) * 0.8)(D, 2 * Fh)), 'w_sp': _w(0.01 * (lambda *s: torch.randn(*s, dtype=torch.bfloat16) * 0.8)(S, S)), 'b_sp': _w(1.0 + 0.01 * (lambda *s: torch.randn(*s, dtype=torch.bfloat16) * 0.8)(S)), 'w_out': _w((lambda *s: torch.randn(*s, dtype=torch.bfloat16) * 0.8)(Fh, D))}
    _B_in = _B_setup()
    return {'A_x': _A_in['x'], 'A_dft_seq_re': _A_in['dft_seq_re'], 'A_dft_seq_im': _A_in['dft_seq_im'], 'A_dft_hid_re': _A_in['dft_hid_re'], 'A_dft_hid_im': _A_in['dft_hid_im'], 'B_x': _B_in['x'], 'B_w_in': _B_in['w_in'], 'B_w_sp': _B_in['w_sp'], 'B_b_sp': _B_in['b_sp'], 'B_w_out': _B_in['w_out'], 'gate_w': _w((lambda *s: torch.randn(*s, dtype=torch.bfloat16) * 0.05)(448) * 16.0)}

def reference(A_x, A_dft_seq_re, A_dft_seq_im, A_dft_hid_re, A_dft_hid_im, B_x, B_w_in, B_w_sp, B_b_sp, B_w_out, gate_w):

    def _A_ref(x, dft_seq_re, dft_seq_im, dft_hid_re, dft_hid_im):
        a = torch.einsum('bsd,ed->bse', x, dft_hid_re)
        b = torch.einsum('bsd,ed->bse', x, dft_hid_im)
        y = torch.einsum('ts,bse->bte', dft_seq_re, a) - torch.einsum('ts,bse->bte', dft_seq_im, b)
        return y

    def _B_ref(x, w_in, w_sp, b_sp, w_out):
        B, S, D = x.shape
        F = w_in.shape[-1]
        Fh = F // 2
        z = _gelu(torch.einsum('bsd,df->bsf', x, w_in))
        z1 = z[..., :Fh].clone()
        z2 = _layernorm(z[..., Fh:], axis=-1)
        g = torch.einsum('ts,bsf->btf', w_sp, z2) + b_sp[None, :, None]
        return torch.einsum('bsf,fd->bsd', z1 * g, w_out)
    x = A_x
    g = _sigmoid((x * gate_w).sum(-1, keepdims=True))
    a = _A_ref(x, A_dft_seq_re, A_dft_seq_im, A_dft_hid_re, A_dft_hid_im)
    b = _B_ref(x, B_w_in, B_w_sp, B_b_sp, B_w_out)
    return g * a + (1 - g) * b
```

```cpp
#include <hip/hip_runtime.h>
#include <hip/hip_bf16.h>
typedef unsigned short u16;
__device__ __forceinline__ float bf2f(u16 v){ return __uint_as_float(((unsigned)v)<<16); }
__device__ __forceinline__ u16 f2bf(float f){ __hip_bfloat16 b = __float2bfloat16(f); return *(u16*)&b; }
constexpr int S=2048, D=448, F=1792, FH=896;

__global__ void gemm_naive(const u16* A, int lda, const u16* B, int sbk, int sbn, float* C, int ldc, int M, int N, int K){
  int n = blockIdx.x*blockDim.x+threadIdx.x, m = blockIdx.y;
  if(n>=N) return;
  float acc=0;
  for(int k=0;k<K;++k) acc += bf2f(A[(size_t)m*lda+k])*bf2f(B[(size_t)k*sbk+(size_t)n*sbn]);
  C[(size_t)m*ldc+n]=acc;
}
__global__ void cvt(const float* in, u16* out, int n){ int i=blockIdx.x*blockDim.x+threadIdx.x; if(i<n) out[i]=f2bf(in[i]); }
__global__ void gelu_ln(const float* zin, u16* z1, u16* z2){
  int s=blockIdx.x; __shared__ float z[F]; __shared__ float red[2];
  for(int f=threadIdx.x;f<F;f+=blockDim.x){ float v=bf2f(f2bf(zin[(size_t)s*F+f])); float t=tanhf(0.7978845608028654f*(v+0.044715f*v*v*v)); z[f]=bf2f(f2bf(0.5f*v*(1.f+t))); }
  __syncthreads();
  if(threadIdx.x==0){ float m=0; for(int f=0;f<FH;++f) m+=z[FH+f]; m/=FH; float q=0; for(int f=0;f<FH;++f){ float d=z[FH+f]-m; q+=d*d;} red[0]=m; red[1]=1.f/sqrtf(q/FH+1e-5f);}
  __syncthreads();
  for(int f=threadIdx.x;f<FH;f+=blockDim.x){ z1[(size_t)s*FH+f]=f2bf(z[f]); z2[(size_t)s*FH+f]=f2bf((z[FH+f]-red[0])*red[1]); }
}
__global__ void hmul(const float* gg, const u16* z1, const u16* bsp, u16* h){ int i=blockIdx.x*blockDim.x+threadIdx.x; if(i<S*FH){ int t=i/FH; h[i]=f2bf(bf2f(z1[i])*(gg[i]+bf2f(bsp[t]))); } }
__global__ void fin(const u16* x, const u16* gw, const float* yre, const float* yim, const float* ob, u16* out){
  int s=blockIdx.x; __shared__ float g;
  if(threadIdx.x==0){ float a=0; for(int d=0;d<D;++d) a+=bf2f(x[s*D+d])*bf2f(gw[d]); g=1.f/(1.f+expf(-a)); }
  __syncthreads();
  for(int d=threadIdx.x;d<D;d+=blockDim.x){ int i=s*D+d; out[i]=f2bf(g*(yre[i]-yim[i])+(1.f-g)*ob[i]); }
}
extern "C" void kernel_launch(void* const* d_in, const int* in_sizes, int n_in, void* d_out, int out_size, void* d_ws, size_t ws_size, hipStream_t stream){
  const u16* x=(const u16*)d_in[0]; const u16* wsr=(const u16*)d_in[1]; const u16* wsi=(const u16*)d_in[2];
  const u16* wdr=(const u16*)d_in[3]; const u16* wdi=(const u16*)d_in[4];
  const u16* win=(const u16*)d_in[6]; const u16* wsp=(const u16*)d_in[7]; const u16* bsp=(const u16*)d_in[8];
  const u16* wout=(const u16*)d_in[9]; const u16* gw=(const u16*)d_in[10];
  char* ws=(char*)d_ws;
  float* f0=(float*)ws; float* f1=(float*)(ws+(16<<20)); float* f2=(float*)(ws+(32<<20)); float* f3=(float*)(ws+(48<<20)); float* f4=(float*)(ws+(64<<20));
  u16* a=(u16*)(ws+(80<<20)); u16* b=(u16*)(ws+(88<<20)); u16* z1=(u16*)(ws+(96<<20)); u16* z2=(u16*)(ws+(104<<20)); u16* h=(u16*)(ws+(112<<20));
  gemm_naive<<<dim3(2,S),256,0,stream>>>(x,D,wdr,1,D,f0,D,S,D,D);
  cvt<<<(S*D+255)/256,256,0,stream>>>(f0,a,S*D);
  gemm_naive<<<dim3(2,S),256,0,stream>>>(x,D,wdi,1,D,f0,D,S,D,D);
  cvt<<<(S*D+255)/256,256,0,stream>>>(f0,b,S*D);
  gemm_naive<<<dim3(2,S),256,0,stream>>>(wsr,S,a,D,1,f1,D,S,D,S);
  gemm_naive<<<dim3(2,S),256,0,stream>>>(wsi,S,b,D,1,f2,D,S,D,S);
  gemm_naive<<<dim3(7,S),256,0,stream>>>(x,D,win,F,1,f0,F,S,F,D);
  gelu_ln<<<S,256,0,stream>>>(f0,z1,z2);
  gemm_naive<<<dim3(4,S),256,0,stream>>>(wsp,S,z2,FH,1,f3,FH,S,FH,S);
  hmul<<<(S*FH+255)/256,256,0,stream>>>(f3,z1,bsp,h);
  gemm_naive<<<dim3(2,S),256,0,stream>>>(h,FH,wout,D,1,f4,D,S,D,FH);
  fin<<<S,256,0,stream>>>(x,gw,f1,f2,f4,(u16*)d_out);
}
```

```cpp
#include <hip/hip_runtime.h>
#include <hip/hip_bf16.h>

typedef unsigned short u16;
typedef unsigned long long u64;
typedef short bf16x8 __attribute__((ext_vector_type(8)));
typedef short bf16x4 __attribute__((ext_vector_type(4)));
typedef float f32x4 __attribute__((ext_vector_type(4)));
typedef unsigned u32x2 __attribute__((ext_vector_type(2)));
typedef unsigned u32x4 __attribute__((ext_vector_type(4)));
#define LAS __attribute__((address_space(3)))
#define GAS __attribute__((address_space(1)))

#define WAITV(n) asm volatile("s_waitcnt vmcnt(%0)" ::"n"(n) : "memory")
#define WAITL0() asm volatile("s_waitcnt lgkmcnt(0)" ::: "memory")
#define BAR() do { asm volatile("" ::: "memory"); __builtin_amdgcn_s_barrier(); asm volatile("" ::: "memory"); } while (0)
#define GLDS(g, l, aux) __builtin_amdgcn_global_load_lds((const GAS void*)(g), (LAS void*)(l), 16, 0, aux)
__device__ __forceinline__ void glds_asm(const void* sbase, unsigned voff, unsigned l) {
  unsigned keep;
  asm volatile("s_mov_b32 %0, m0\n\ts_mov_b32 m0, %3\n\ts_nop 0\n\tglobal_load_lds_dwordx4 %1, %2\n\ts_mov_b32 m0, %0"
               : "=&s"(keep) : "v"(voff), "s"(sbase), "s"(l) : "memory");
}
#define MFMA(a, b, c) __builtin_amdgcn_mfma_f32_16x16x32_bf16(a, b, c, 0, 0, 0)

#define AUX_A 2

constexpr int S = 2048, D = 448, F = 1792, FH = 896;

__device__ __forceinline__ float bf2f(u16 v) { return __uint_as_float(((unsigned)v) << 16); }
__device__ __forceinline__ float bflo(unsigned v) { return __uint_as_float(v << 16); }
__device__ __forceinline__ float bfhi(unsigned v) { return __uint_as_float(v & 0xffff0000u); }
__device__ __forceinline__ unsigned pk_bf16(float lo, float hi) {
  typedef __bf16 bf2 __attribute__((ext_vector_type(2)));
  typedef float f2 __attribute__((ext_vector_type(2)));
  f2 v = {lo, hi};
  bf2 b = __builtin_convertvector(v, bf2);
  return *(unsigned*)&b;
}
__device__ __forceinline__ float gelu(float v) {
  const float c0 = -2.0f * 0.7978845608028654f * 1.4426950408889634f;
  const float c1 = c0 * 0.044715f;
  float e = __builtin_amdgcn_exp2f(v * __builtin_fmaf(v * v, c1, c0));
  return v * __builtin_amdgcn_rcpf(1.0f + e);
}

constexpr size_t OFF_BT = 0;
constexpr size_t OFF_O2 = OFF_BT + (size_t)1792 * S * 2;
constexpr size_t OFF_Z1 = OFF_O2 + (size_t)1792 * S * 2;
constexpr size_t OFF_WO = OFF_Z1 + (size_t)896 * S * 2;
constexpr size_t OFF_G = OFF_WO + (size_t)896 * 448 * 2;
constexpr size_t OFF_X = OFF_G + 8192;
constexpr size_t OFF_E = OFF_X + (size_t)64 * 14 * 512;
constexpr u64 EPOCH_STEP = 0x9E3779B97F4A7C15ull;

__global__ __launch_bounds__(512, 2) void k1(const u16* __restrict__ x, const u16* __restrict__ wdr, const u16* __restrict__ wdi,
                                             const u16* __restrict__ win, const u16* __restrict__ wout, const u16* __restrict__ gw,
                                             char* __restrict__ ws) {
  __shared__ __attribute__((aligned(1024))) char smem[5 * 32768];
  LAS char* lds = (LAS char*)smem;
  const int wid = __builtin_amdgcn_readfirstlane(threadIdx.x >> 6), lane = threadIdx.x & 63;
  const int b = blockIdx.x, xcd = b & 7, r = b >> 3;
  if (r >= 28) {
    const int sj = xcd * 4 + (r - 28);
    {
      u32x4 gv = {0, 0, 0, 0}, xv[8];
      if (lane < 56) gv = *(const u32x4*)(gw + lane * 8);
      const int row0 = sj * 64 + wid * 8;
#pragma unroll
      for (int i = 0; i < 8; ++i) {
        xv[i] = (u32x4){0, 0, 0, 0};
        if (lane < 56) xv[i] = *(const u32x4*)(x + (size_t)(row0 + i) * D + lane * 8);
      }
      float res = 0.f;
#pragma unroll
      for (int i = 0; i < 8; ++i) {
        float a = 0.f;
#pragma unroll
        for (int j = 0; j < 4; ++j) a += bflo(xv[i][j]) * bflo(gv[j]) + bfhi(xv[i][j]) * bfhi(gv[j]);
#pragma unroll
        for (int o = 32; o >= 1; o >>= 1) a += __shfl_xor(a, o);
        if (lane == i) res = a;
      }
      if (lane < 8) ((float*)(ws + OFF_G))[row0 + lane] = 1.0f / (1.0f + __expf(-res));
    }
    const int rr = lane & 15, q = lane >> 4;
    for (int blk = sj * 8 + wid; blk < 784; blk += 256) {
      const int t4 = blk & 3, kb = (blk >> 2) % 28, dg = (blk >> 2) / 28;
      const int d = 64 * dg + 32 * (t4 >> 1) + 8 * (rr >> 2) + 4 * (t4 & 1) + (rr & 3);
      unsigned v[8];
#pragma unroll
      for (int e = 0; e < 8; ++e) v[e] = wout[(size_t)(32 * kb + 16 * (e >> 2) + 4 * q + (e & 3)) * D + d];
      u32x4 o = {v[0] | (v[1] << 16), v[2] | (v[3] << 16), v[4] | (v[5] << 16), v[6] | (v[7] << 16)};
      *(u32x4*)(ws + OFF_WO + (size_t)blk * 1024 + lane * 16) = o;
    }
    return;
  }
  const int rp = 4 * (xcd >> 1) + (r & 3), cg = 7 * (xcd & 1) + (r >> 2);
  const int w = wid & 3, half = wid >> 2;
  const unsigned ldsb = (unsigned)(size_t)lds;
  const unsigned l3 = lane >> 3, l7 = lane & 7, l4 = (lane >> 4) & 1;
  const char* xg = (const char*)x + (size_t)(rp * 128 + wid * 16) * 896;
  const unsigned xo0 = l3 * 896 + ((l7 ^ (l4 | (((2 * wid) & 3) << 1))) << 4);
  const unsigned xo1 = (l3 + 8) * 896 + ((l7 ^ (l4 | (((2 * wid + 1) & 3) << 1))) << 4);
  const char* wg = (const char*)win + (size_t)(wid * 8) * 3584 + cg * 128;
  const unsigned wo = l3 * 3584 + (((((l7 >> 1) ^ (l4 | ((wid & 1) << 1))) << 1) | (l7 & 1)) << 4);
  const char* dgp = (const char*)(cg < 7 ? wdr : wdi) + (size_t)((cg < 7 ? cg : cg - 7) * 64 + wid * 8) * 896;
  const unsigned dofs = l3 * 896 + ((l7 ^ (l4 | ((wid & 3) << 1))) << 4);
  auto stage = [&](int st, int slot) {
    const unsigned l = ldsb + slot * 32768;
    glds_asm(xg + st * 128, xo0, l + wid * 2048);
    glds_asm(xg + st * 128, xo1, l + wid * 2048 + 1024);
    glds_asm(wg + (size_t)st * 64 * 3584, wo, l + 16384 + wid * 1024);
    glds_asm(wg + (size_t)st * 64 * 3584 + 1792, wo, l + 24576 + wid * 1024);
  };
  auto stage_d = [&](int c) { glds_asm(dgp + c * 128, dofs, ldsb + 65536 + c * 8192 + wid * 1024); };
#pragma unroll
  for (int s = 0; s < 5; ++s) stage(s, s);
  const int grp = rp * 4 + w;
  const u64 tag = (*(const u64*)(ws + OFF_E + grp * 128) + EPOCH_STEP);
  const int fi = lane & 15, fq = lane >> 4;
  const unsigned arow = 8 * (fi >> 2) + (fi & 3);
  const unsigned ax0 = (32 * w + arow) * 128 + ((fq ^ (fi >> 1)) << 4);
  const unsigned ax1 = (32 * w + arow) * 128 + (((fq + 4) ^ (fi >> 1)) << 4);
  const unsigned tq = fi >> 2, tp = lane & 3, fw = ((tq >> 1) & 1) | ((fq & 1) << 1);
  unsigned bw[4];
#pragma unroll
  for (int n = 0; n < 4; ++n) bw[n] = (half ? 16384 : 24576) + (8 * fq + tq) * 128 + ((n ^ fw) << 5) + tp * 8;
  bf16x8 xf[14][2];
  f32x4 az[2][4];
#pragma unroll
  for (int h = 0; h < 2; ++h)
#pragma unroll
    for (int n = 0; n < 4; ++n) az[h][n] = (f32x4){0, 0, 0, 0};
#pragma unroll
  for (int t = 0; t < 7; ++t) {
    if (t == 0) WAITV(16); else if (t < 5) WAITV(12); else if (t == 5) WAITV(11); else WAITV(7);
    WAITL0();
    BAR();
    if (t == 1 || t == 2) stage(t + 4, t - 1);
    if (t == 3) { stage_d(0); stage_d(1); stage_d(2); stage_d(3); }
    if (t == 4) { stage_d(4); stage_d(5); stage_d(6); }
    LAS char* l = lds + (t % 5) * 32768;
#pragma unroll
    for (int k = 0; k < 2; ++k) {
#pragma unroll
      for (int h = 0; h < 2; ++h) xf[2 * t + k][h] = *(const LAS bf16x8*)(l + (k ? ax1 : ax0) + h * 512);
      bf16x8 wf[4];
#pragma unroll
      for (int n = 0; n < 4; ++n) {
        bf16x4 lo = __builtin_amdgcn_ds_read_tr16_b64_v4i16((LAS bf16x4*)(l + bw[n] + k * 4096));
        bf16x4 hi = __builtin_amdgcn_ds_read_tr16_b64_v4i16((LAS bf16x4*)(l + bw[n] + k * 4096 + 512));
        wf[n] = __builtin_shufflevector(lo, hi, 0, 1, 2, 3, 4, 5, 6, 7);
      }
#pragma unroll
      for (int n = 0; n < 4; ++n)
#pragma unroll
        for (int h = 0; h < 2; ++h) az[h][n] = MFMA(wf[n], xf[2 * t + k][h], az[h][n]);
    }
  }
  WAITV(0);
  WAITL0();
  BAR();
  auto pass2 = [&]() {
    f32x4 ab[2][2];
#pragma unroll
    for (int h = 0; h < 2; ++h)
#pragma unroll
      for (int n = 0; n < 2; ++n) ab[h][n] = (f32x4){0, 0, 0, 0};
    const unsigned d0 = 65536 + (32 * half + arow) * 128 + ((fq ^ (fi >> 1)) << 4);
    const unsigned d1 = 65536 + (32 * half + arow) * 128 + (((fq + 4) ^ (fi >> 1)) << 4);
#pragma unroll
    for (int ks = 0; ks < 14; ++ks) {
      bf16x8 df[2];
#pragma unroll
      for (int n = 0; n < 2; ++n) df[n] = *(const LAS bf16x8*)(lds + ((ks & 1) ? d1 : d0) + (ks >> 1) * 8192 + n * 512);
#pragma unroll
      for (int n = 0; n < 2; ++n)
#pragma unroll
        for (int h = 0; h < 2; ++h) ab[h][n] = MFMA(xf[ks][h], df[n], ab[h][n]);
    }
#pragma unroll
    for (int n = 0; n < 2; ++n) {
      const int T = 4 * cg + 2 * half + n, p = T / 7, n7 = T % 7;
      u32x4 o = {pk_bf16(ab[0][n][0], ab[0][n][1]), pk_bf16(ab[0][n][2], ab[0][n][3]),
                 pk_bf16(ab[1][n][0], ab[1][n][1]), pk_bf16(ab[1][n][2], ab[1][n][3])};
      *(u32x4*)(ws + OFF_BT + ((size_t)(p * 64 + 4 * rp + w) * 7 + n7) * 1024 + lane * 16) = o;
    }
  };
  auto do_gelu = [&]() {
#pragma unroll
    for (int h = 0; h < 2; ++h)
#pragma unroll
      for (int n = 0; n < 4; ++n)
#pragma unroll
        for (int j = 0; j < 4; ++j) az[h][n][j] = gelu(az[h][n][j]);
  };
  if (half) {
    pass2();
    do_gelu();
#pragma unroll
    for (int h = 0; h < 2; ++h)
#pragma unroll
      for (int k = 0; k < 2; ++k) {
        u32x4 o = {pk_bf16(az[h][2 * k][0], az[h][2 * k][1]), pk_bf16(az[h][2 * k][2], az[h][2 * k][3]),
                   pk_bf16(az[h][2 * k + 1][0], az[h][2 * k + 1][1]), pk_bf16(az[h][2 * k + 1][2], az[h][2 * k + 1][3])};
        *(u32x4*)(ws + OFF_Z1 + ((size_t)(8 * rp + 2 * w + h) * 28 + 2 * cg + k) * 1024 + lane * 16) = o;
      }
    return;
  }
  do_gelu();
  float mean_p[2], m2_p[2];
#pragma unroll
  for (int h = 0; h < 2; ++h) {
    float s = 0.f;
#pragma unroll
    for (int n = 0; n < 4; ++n) s += (az[h][n][0] + az[h][n][1]) + (az[h][n][2] + az[h][n][3]);
    s += __shfl_xor(s, 16);
    s += __shfl_xor(s, 32);
    mean_p[h] = s * (1.0f / 64.0f);
  }
#pragma unroll
  for (int h = 0; h < 2; ++h) {
    float q = 0.f;
#pragma unroll
    for (int n = 0; n < 4; ++n)
#pragma unroll
      for (int j = 0; j < 4; ++j) { float d = az[h][n][j] - mean_p[h]; q = __builtin_fmaf(d, d, q); }
    q += __shfl_xor(q, 16);
    q += __shfl_xor(q, 32);
    m2_p[h] = q;
  }
  GAS u64* xch = (GAS u64*)(ws + OFF_X) + (size_t)grp * 14 * 64 + lane;
  const u64 tag40 = tag << 24;
  {
    const float val = (fq & 2) ? ((fq & 1) ? m2_p[1] : m2_p[0]) : ((fq & 1) ? mean_p[1] : mean_p[0]);
    const unsigned bits = (__float_as_uint(val) + 0x80u) >> 8;
    __hip_atomic_store(xch + cg * 64, tag40 | bits, __ATOMIC_RELAXED, __HIP_MEMORY_SCOPE_AGENT);
  }
  pass2();
  float pv[14];
  for (unsigned spins = 0;; ++spins) {
    bool ok = true;
#pragma unroll
    for (int c = 0; c < 14; ++c) {
      const u64 g = __hip_atomic_load(xch + c * 64, __ATOMIC_RELAXED, __HIP_MEMORY_SCOPE_AGENT);
      pv[c] = __uint_as_float((unsigned)g << 8);
      ok &= ((g ^ tag40) >> 24) == 0;
    }
    if (__all(ok) || spins > (1u << 22)) break;
    __builtin_amdgcn_s_sleep(1);
  }
  if (cg == 0 && lane == 0) __hip_atomic_store((GAS u64*)(ws + OFF_E + grp * 128), tag, __ATOMIC_RELAXED, __HIP_MEMORY_SCOPE_AGENT);
  float sum = 0.f;
#pragma unroll
  for (int c = 0; c < 14; ++c) sum += pv[c];
  const float mean = sum * (1.0f / 14.0f);
  float dev = 0.f;
#pragma unroll
  for (int c = 0; c < 14; ++c) { float d = pv[c] - mean; dev = __builtin_fmaf(d, d, dev); }
  const float mine = (fq & 2) ? sum : dev * 64.0f;
  const float m2 = mine + __shfl_xor(mine, 32);
  const float rstd = __builtin_amdgcn_rsqf(m2 * (1.0f / 896.0f) + 1e-5f);
  float mu[2], rs[2];
#pragma unroll
  for (int h = 0; h < 2; ++h) {
    mu[h] = __shfl(mean, fi + 16 * h);
    rs[h] = __shfl(rstd, fi + 16 * h);
  }
  LAS char* sc = lds + 4 * 32768 + w * 4096;
#pragma unroll
  for (int h = 0; h < 2; ++h)
#pragma unroll
    for (int n = 0; n < 4; ++n) {
      u32x2 o = {pk_bf16((az[h][n][0] - mu[h]) * rs[h], (az[h][n][1] - mu[h]) * rs[h]),
                 pk_bf16((az[h][n][2] - mu[h]) * rs[h], (az[h][n][3] - mu[h]) * rs[h])};
      *(LAS u32x2*)(sc + (arow + 4 * h) * 128 + (((4 * n + fq) ^ fi) << 3)) = o;
    }
  WAITL0();
#pragma unroll
  for (int n = 0; n < 4; ++n) {
    const unsigned a = (8 * fq + tq) * 128 + (((4 * n + tp) ^ ((fq << 2) | tq)) << 3);
    bf16x4 lo = __builtin_amdgcn_ds_read_tr16_b64_v4i16((LAS bf16x4*)(sc + a));
    bf16x4 hi = __builtin_amdgcn_ds_read_tr16_b64_v4i16((LAS bf16x4*)(sc + a + 512));
    bf16x8 o = __builtin_shufflevector(lo, hi, 0, 1, 2, 3, 4, 5, 6, 7);
    const int T = 56 + 4 * cg + n, p = T / 7, n7 = T % 7;
    *(bf16x8*)(ws + OFF_BT + ((size_t)(p * 64 + 4 * rp + w) * 7 + n7) * 1024 + lane * 16) = o;
  }
}

constexpr int STA = 16384, STB = 14336, STG = STA + STB, RING = 5, NT = 32;

template <int P> __device__ __forceinline__ void waitv_tail(int n) {
  switch (n) {
    case 0: WAITV(0); break;
    case 1: WAITV(P); break;
    case 2: WAITV(2 * P); break;
    default: WAITV(3 * P); break;
  }
}

template <int ODD>
__device__ __forceinline__ void k2_loadz(const char* zp, u32x4 (&zq)[2][3], u32x2 (&zs)[2]) {
#pragma unroll
  for (int m = 0; m < 2; ++m) {
    const char* zm = zp + (size_t)m * 28 * 1024;
#pragma unroll
    for (int k = 0; k < 3; ++k) zq[m][k] = *(const u32x4*)(zm + (k + ODD) * 1024);
    zs[m] = *(const u32x2*)(zm + (ODD ? 8 : 3 * 1024));
  }
}
template <int ODD>
__device__ __forceinline__ void k2_epi(f32x4 (&acc)[2][7], bool gate, const float (&bb)[2], const u32x4 (&zq)[2][3],
                                       const u32x2 (&zs)[2], char* op) {
#pragma unroll
  for (int m = 0; m < 2; ++m) {
    if (gate) {
#pragma unroll
      for (int n = 0; n < 7; ++n) {
        const bool single = ODD ? (n == 0) : (n == 6);
        const int nn = n - ODD;
        const unsigned z0 = single ? zs[m][0] : zq[m][(nn < 0 ? 0 : nn) >> 1 < 3 ? (nn < 0 ? 0 : nn) >> 1 : 0][(nn & 1) * 2];
        const unsigned z1 = single ? zs[m][1] : zq[m][(nn < 0 ? 0 : nn) >> 1 < 3 ? (nn < 0 ? 0 : nn) >> 1 : 0][(nn & 1) * 2 + 1];
        acc[m][n][0] = (acc[m][n][0] + bb[m]) * bflo(z0);
        acc[m][n][1] = (acc[m][n][1] + bb[m]) * bfhi(z0);
        acc[m][n][2] = (acc[m][n][2] + bb[m]) * bflo(z1);
        acc[m][n][3] = (acc[m][n][3] + bb[m]) * bfhi(z1);
      }
    }
    char* om = op + (size_t)m * 56 * 1024;
#pragma unroll
    for (int k = 0; k < 3; ++k) {
      const f32x4 a = acc[m][2 * k + ODD], c = acc[m][2 * k + 1 + ODD];
      u32x4 o = {pk_bf16(a[0], a[1]), pk_bf16(a[2], a[3]), pk_bf16(c[0], c[1]), pk_bf16(c[2], c[3])};
      *(u32x4*)(om + (k + ODD) * 1024) = o;
    }
    const f32x4 a = acc[m][ODD ? 0 : 6];
    u32x2 o = {pk_bf16(a[0], a[1]), pk_bf16(a[2], a[3])};
    *(u32x2*)(om + (ODD ? 8 : 3 * 1024)) = o;
  }
}

__global__ __launch_bounds__(512, 2) void k2(const u16* __restrict__ wsr, const u16* __restrict__ wsi, const u16* __restrict__ wsp,
                                             const u16* __restrict__ bsp, char* __restrict__ ws) {
  __shared__ __attribute__((aligned(1024))) char smem[RING * STG];
  LAS char* lds = (LAS char*)smem;
  const int wid = __builtin_amdgcn_readfirstlane(threadIdx.x >> 6), lane = threadIdx.x & 63;
  const int b = blockIdx.x, xcd = b & 7, r = b >> 3;
  int mi, p;
  if (xcd < 4) { mi = xcd * 4 + (r & 3); p = 8 + (r >> 2); }
  else { const int h = xcd - 4; mi = (h & 1) * 8 + (r & 7); p = (h >> 1) * 4 + (r >> 3); }
  if (wid >= 4) {
    const int lw = wid - 4;
    if (lw < 2) {
      const u16* A = p < 4 ? wsr : (p < 8 ? wsi : wsp);
      const char* base = (const char*)A + (size_t)(mi * 128 + lw * 64) * 4096;
      unsigned off[4];
#pragma unroll
      for (int i = 0; i < 4; ++i) off[i] = (unsigned)((lane >> 3) * 4096 + i * 32768 + (((lane & 7) ^ (((lane >> 4) & 1) | (i << 1))) << 4));
      auto issue = [&](int st, int slot) {
        const char* g = base + st * 128;
        LAS char* l = lds + slot * STG + lw * 8192;
#pragma unroll
        for (int i = 0; i < 8; ++i) GLDS(g + (size_t)(i >> 2) * 131072 + off[i & 3], l + i * 1024, AUX_A);
      };
#pragma unroll
      for (int s = 0; s < RING; ++s) issue(s, s);
      WAITV(8 * (RING - 1));
      BAR();
      int slot = 0;
      for (int t = 0; t < NT - RING; ++t) {
        WAITV(8 * (RING - 2));
        BAR();
        issue(t + RING, slot);
        slot = (slot == RING - 1) ? 0 : slot + 1;
      }
#pragma unroll
      for (int t = NT - RING; t < NT - 1; ++t) { waitv_tail<8>(NT - 2 - t); BAR(); }
    } else {
      const char* base = ws + OFF_BT + (size_t)p * 64 * 7 * 1024 + (lw - 2) * 7168;
      const unsigned off = lane * 16;
      auto issue = [&](int st, int slot) {
        const char* g = base + (size_t)st * STB;
        LAS char* l = lds + slot * STG + STA + (lw - 2) * 7168;
#pragma unroll
        for (int i = 0; i < 7; ++i) GLDS(g + i * 1024 + off, l + i * 1024, 0);
      };
#pragma unroll
      for (int s = 0; s < RING; ++s) issue(s, s);
      WAITV(7 * (RING - 1));
      BAR();
      int slot = 0;
      for (int t = 0; t < NT - RING; ++t) {
        WAITV(7 * (RING - 2));
        BAR();
        issue(t + RING, slot);
        slot = (slot == RING - 1) ? 0 : slot + 1;
      }
#pragma unroll
      for (int t = NT - RING; t < NT - 1; ++t) { waitv_tail<7>(NT - 2 - t); BAR(); }
    }
    return;
  }
  const int fi = lane & 15, fq = lane >> 4;
  const unsigned arow = 8 * (fi >> 2) + (fi & 3);
  const unsigned aoff0 = (32 * wid + arow) * 128 + ((fq ^ (fi >> 1)) << 4);
  const unsigned aoff1 = (32 * wid + arow) * 128 + (((fq + 4) ^ (fi >> 1)) << 4);
  const unsigned boff = STA + lane * 16;
  const int mt0 = mi * 8 + wid * 2;
  const bool gate = p >= 8;
  u32x4 zq[2][3];
  u32x2 zs[2];
  float bb[2] = {0.f, 0.f};
  if (gate) {
    const char* zp = ws + OFF_Z1 + ((size_t)mt0 * 28 + ((7 * (p - 8)) >> 1)) * 1024 + lane * 16;
    if (p & 1) k2_loadz<1>(zp, zq, zs); else k2_loadz<0>(zp, zq, zs);
    bb[0] = bf2f(bsp[mi * 128 + 32 * wid + arow]);
    bb[1] = bf2f(bsp[mi * 128 + 32 * wid + arow + 4]);
  }
  f32x4 acc[2][7];
#pragma unroll
  for (int m = 0; m < 2; ++m)
#pragma unroll
    for (int n = 0; n < 7; ++n) acc[m][n] = (f32x4){0, 0, 0, 0};
  bf16x8 fa[2][2][2], fb[2][2][7];
  auto rd = [&](int set, int slot) {
    LAS char* l = lds + slot * STG;
#pragma unroll
    for (int k = 0; k < 2; ++k) {
#pragma unroll
      for (int m = 0; m < 2; ++m) fa[set][k][m] = *(const LAS bf16x8*)(l + (k ? aoff1 : aoff0) + m * 512);
#pragma unroll
      for (int n = 0; n < 7; ++n) fb[set][k][n] = *(const LAS bf16x8*)(l + boff + k * 7168 + n * 1024);
    }
  };
  auto mm = [&](int set) {
#pragma unroll
    for (int k = 0; k < 2; ++k)
#pragma unroll
      for (int n = 0; n < 7; ++n)
#pragma unroll
        for (int m = 0; m < 2; ++m) acc[m][n] = MFMA(fb[set][k][n], fa[set][k][m], acc[m][n]);
  };
  auto sched = [&]() {
#pragma unroll
    for (int i = 0; i < 18; ++i) {
      __builtin_amdgcn_sched_group_barrier(0x8, 1, 0);
      __builtin_amdgcn_sched_group_barrier(0x100, 1, 0);
    }
    __builtin_amdgcn_sched_group_barrier(0x8, 10, 0);
    __builtin_amdgcn_sched_barrier(0);
  };
  BAR();
  rd(0, 0);
  int slot = 1;
  for (int t = 0; t < NT - 2; t += 2) {
    WAITL0(); BAR();
    rd(1, slot); mm(0); sched();
    slot = (slot == RING - 1) ? 0 : slot + 1;
    WAITL0(); BAR();
    rd(0, slot); mm(1); sched();
    slot = (slot == RING - 1) ? 0 : slot + 1;
  }
  WAITL0(); BAR();
  rd(1, slot); mm(0); sched();
  mm(1);
  char* op = ws + OFF_O2 + ((size_t)mt0 * 56 + ((7 * p) >> 1)) * 1024 + lane * 16;
  if (p & 1) k2_epi<1>(acc, gate, bb, zq, zs, op); else k2_epi<0>(acc, gate, bb, zq, zs, op);
}

__global__ __launch_bounds__(512, 2) void k3(const char* __restrict__ ws, u16* __restrict__ out) {
  __shared__ __attribute__((aligned(1024))) char smem[112 * 1024];
  LAS char* lds = (LAS char*)smem;
  const int wid = __builtin_amdgcn_readfirstlane(threadIdx.x >> 6), lane = threadIdx.x & 63;
  const int b = blockIdx.x, xcd = b & 7, r = b >> 3;
  const int rg = 4 * xcd + (r & 3), dg = r >> 2;
  if (wid >= 4) {
    const int lw = wid - 4;
    const char* g = ws + OFF_WO + (size_t)dg * 112 * 1024 + lw * 1024 + lane * 16;
#pragma unroll
    for (int kb = 0; kb < 28; ++kb) GLDS(g + kb * 4096, lds + kb * 4096 + lw * 1024, 0);
    WAITV(24); BAR();
    WAITV(20); BAR();
    WAITV(16); BAR();
    WAITV(12); BAR();
    WAITV(8); BAR();
    WAITV(4); BAR();
    WAITV(0); BAR();
    return;
  }
  const int mt = 4 * rg + wid, fi = lane & 15, fq = lane >> 4;
  const char* o2 = ws + OFF_O2 + (size_t)mt * 56 * 1024 + lane * 16;
  bf16x8 hf[28];
#pragma unroll
  for (int kb = 0; kb < 28; ++kb) hf[kb] = *(const bf16x8*)(o2 + (28 + kb) * 1024);
  u32x4 yr[2], yi[2];
#pragma unroll
  for (int k = 0; k < 2; ++k) {
    yr[k] = *(const u32x4*)(o2 + (2 * dg + k) * 1024);
    yi[k] = *(const u32x4*)(o2 + (14 + 2 * dg + k) * 1024);
  }
  const int t = 32 * (mt >> 1) + 8 * (fi >> 2) + 4 * (mt & 1) + (fi & 3);
  const float g = ((const float*)(ws + OFF_G))[t];
  __builtin_amdgcn_sched_barrier(0);
  f32x4 acc[4];
#pragma unroll
  for (int n = 0; n < 4; ++n) acc[n] = (f32x4){0, 0, 0, 0};
#pragma unroll
  for (int c = 0; c < 7; ++c) {
    BAR();
#pragma unroll
    for (int k = 0; k < 4; ++k) {
      const int kb = 4 * c + k;
#pragma unroll
      for (int n = 0; n < 4; ++n) {
        const bf16x8 wf = *(const LAS bf16x8*)(lds + kb * 4096 + n * 1024 + lane * 16);
        acc[n] = MFMA(wf, hf[kb], acc[n]);
      }
    }
  }
  const float g1 = 1.0f - g;
#pragma unroll
  for (int k = 0; k < 2; ++k) {
    float v[8];
#pragma unroll
    for (int e = 0; e < 4; ++e) {
      const float a0 = acc[2 * k + (e >> 1)][(e & 1) * 2], a1 = acc[2 * k + (e >> 1)][(e & 1) * 2 + 1];
      v[2 * e] = g * (bflo(yr[k][e]) - bflo(yi[k][e])) + g1 * a0;
      v[2 * e + 1] = g * (bfhi(yr[k][e]) - bfhi(yi[k][e])) + g1 * a1;
    }
    u32x4 o = {pk_bf16(v[0], v[1]), pk_bf16(v[2], v[3]), pk_bf16(v[4], v[5]), pk_bf16(v[6], v[7])};
    *(u32x4*)(out + (size_t)t * D + 64 * dg + 32 * k + 8 * fq) = o;
  }
}

extern "C" void kernel_launch(void* const* d_in, const int* in_sizes, int n_in, void* d_out, int out_size, void* d_ws,
                              size_t ws_size, hipStream_t stream) {
  const u16* x = (const u16*)d_in[0];
  const u16* wsr = (const u16*)d_in[1];
  const u16* wsi = (const u16*)d_in[2];
  const u16* wdr = (const u16*)d_in[3];
  const u16* wdi = (const u16*)d_in[4];
  const u16* win = (const u16*)d_in[6];
  const u16* wsp = (const u16*)d_in[7];
  const u16* bsp = (const u16*)d_in[8];
  const u16* wout = (const u16*)d_in[9];
  const u16* gw = (const u16*)d_in[10];
  char* ws = (char*)d_ws;
  k1<<<256, 512, 0, stream>>>(x, wdr, wdi, win, wout, gw, ws);
  k2<<<256, 512, 0, stream>>>(wsr, wsi, wsp, bsp, ws);
  k3<<<224, 512, 0, stream>>>(ws, (u16*)d_out);
}
```

```cpp
#include <hip/hip_runtime.h>
#include <hip/hip_bf16.h>

typedef unsigned short u16;
typedef unsigned long long u64;
typedef short bf16x8 __attribute__((ext_vector_type(8)));
typedef short bf16x4 __attribute__((ext_vector_type(4)));
typedef float f32x4 __attribute__((ext_vector_type(4)));
typedef unsigned u32x2 __attribute__((ext_vector_type(2)));
typedef unsigned u32x4 __attribute__((ext_vector_type(4)));
#define LAS __attribute__((address_space(3)))
#define GAS __attribute__((address_space(1)))

#define WAITV(n) asm volatile("s_waitcnt vmcnt(%0)" ::"n"(n) : "memory")
#define WAITL0() asm volatile("s_waitcnt lgkmcnt(0)" ::: "memory")
#define BAR() do { asm volatile("" ::: "memory"); __builtin_amdgcn_s_barrier(); asm volatile("" ::: "memory"); } while (0)
#define GLDS(g, l, aux) __builtin_amdgcn_global_load_lds((const GAS void*)(g), (LAS void*)(l), 16, 0, aux)
__device__ __forceinline__ void glds_asm(const void* sbase, unsigned voff, unsigned l) {
  unsigned keep;
  asm volatile("s_mov_b32 %0, m0\n\ts_mov_b32 m0, %3\n\ts_nop 0\n\tglobal_load_lds_dwordx4 %1, %2\n\ts_mov_b32 m0, %0"
               : "=&s"(keep) : "v"(voff), "s"(sbase), "s"(l) : "memory");
}
#define MFMA(a, b, c) __builtin_amdgcn_mfma_f32_16x16x32_bf16(a, b, c, 0, 0, 0)

#define AUX_A 2

constexpr int S = 2048, D = 448, F = 1792, FH = 896;

__device__ __forceinline__ float bf2f(u16 v) { return __uint_as_float(((unsigned)v) << 16); }
__device__ __forceinline__ float bflo(unsigned v) { return __uint_as_float(v << 16); }
__device__ __forceinline__ float bfhi(unsigned v) { return __uint_as_float(v & 0xffff0000u); }
__device__ __forceinline__ unsigned pk_bf16(float lo, float hi) {
  typedef __bf16 bf2 __attribute__((ext_vector_type(2)));
  typedef float f2 __attribute__((ext_vector_type(2)));
  f2 v = {lo, hi};
  bf2 b = __builtin_convertvector(v, bf2);
  return *(unsigned*)&b;
}
__device__ __forceinline__ float gelu(float v) {
  const float c0 = -2.0f * 0.7978845608028654f * 1.4426950408889634f;
  const float c1 = c0 * 0.044715f;
  float e = __builtin_amdgcn_exp2f(v * __builtin_fmaf(v * v, c1, c0));
  return v * __builtin_amdgcn_rcpf(1.0f + e);
}

constexpr size_t OFF_BT = 0;
constexpr size_t OFF_O2 = OFF_BT + (size_t)1792 * S * 2;
constexpr size_t OFF_Z1 = OFF_O2 + (size_t)1792 * S * 2;
constexpr size_t OFF_WO = OFF_Z1 + (size_t)896 * S * 2;
constexpr size_t OFF_G = OFF_WO + (size_t)896 * 448 * 2;
constexpr size_t OFF_X = OFF_G + 8192;
constexpr size_t OFF_E = OFF_X + (size_t)64 * 14 * 512;
#define STAMP_DECL
#define STAMP(i)
#define STAMP_FLUSH(k)
constexpr u64 EPOCH_STEP = 0x9E3779B97F4A7C15ull;

__global__ __launch_bounds__(512, 2) void k1(const u16* __restrict__ x, const u16* __restrict__ wdr, const u16* __restrict__ wdi,
                                             const u16* __restrict__ win, const u16* __restrict__ wout, const u16* __restrict__ gw,
                                             char* __restrict__ ws) {
  __shared__ __attribute__((aligned(1024))) char smem[5 * 32768];
  LAS char* lds = (LAS char*)smem;
  const int wid = __builtin_amdgcn_readfirstlane(threadIdx.x >> 6), lane = threadIdx.x & 63;
  STAMP_DECL; STAMP(0);
  const int b = blockIdx.x, xcd = b & 7, r = b >> 3;
  if (r >= 28) {
    const int sj = xcd * 4 + (r - 28);
    {
      u32x4 gv = {0, 0, 0, 0}, xv[8];
      if (lane < 56) gv = *(const u32x4*)(gw + lane * 8);
      const int row0 = sj * 64 + wid * 8;
#pragma unroll
      for (int i = 0; i < 8; ++i) {
        xv[i] = (u32x4){0, 0, 0, 0};
        if (lane < 56) xv[i] = *(const u32x4*)(x + (size_t)(row0 + i) * D + lane * 8);
      }
      float res = 0.f;
#pragma unroll
      for (int i = 0; i < 8; ++i) {
        float a = 0.f;
#pragma unroll
        for (int j = 0; j < 4; ++j) a += bflo(xv[i][j]) * bflo(gv[j]) + bfhi(xv[i][j]) * bfhi(gv[j]);
#pragma unroll
        for (int o = 32; o >= 1; o >>= 1) a += __shfl_xor(a, o);
        if (lane == i) res = a;
      }
      if (lane < 8) ((float*)(ws + OFF_G))[row0 + lane] = 1.0f / (1.0f + __expf(-res));
    }
    STAMP(1);
    const int rr = lane & 15, q = lane >> 4;
    for (int blk = sj * 8 + wid; blk < 784; blk += 256) {
      const int t4 = blk & 3, kb = (blk >> 2) % 28, dg = (blk >> 2) / 28;
      const int d = 64 * dg + 32 * (t4 >> 1) + 8 * (rr >> 2) + 4 * (t4 & 1) + (rr & 3);
      unsigned v[8];
#pragma unroll
      for (int e = 0; e < 8; ++e) v[e] = wout[(size_t)(32 * kb + 16 * (e >> 2) + 4 * q + (e & 3)) * D + d];
      u32x4 o = {v[0] | (v[1] << 16), v[2] | (v[3] << 16), v[4] | (v[5] << 16), v[6] | (v[7] << 16)};
      *(u32x4*)(ws + OFF_WO + (size_t)blk * 1024 + lane * 16) = o;
    }
    STAMP_FLUSH(0);
    return;
  }
  const int rp = 4 * (xcd >> 1) + (r & 3), cg = 7 * (xcd & 1) + (r >> 2);
  const int w = wid & 3, half = wid >> 2;
  const unsigned ldsb = (unsigned)(size_t)lds;
  const unsigned l3 = lane >> 3, l7 = lane & 7, l4 = (lane >> 4) & 1;
  const char* xg = (const char*)x + (size_t)(rp * 128 + wid * 16) * 896;
  const unsigned xo0 = l3 * 896 + ((l7 ^ (l4 | (((2 * wid) & 3) << 1))) << 4);
  const unsigned xo1 = (l3 + 8) * 896 + ((l7 ^ (l4 | (((2 * wid + 1) & 3) << 1))) << 4);
  const char* wg = (const char*)win + (size_t)(wid * 8) * 3584 + cg * 128;
  const unsigned wo = l3 * 3584 + (((((l7 >> 1) ^ (l4 | ((wid & 1) << 1))) << 1) | (l7 & 1)) << 4);
  const char* dgp = (const char*)(cg < 7 ? wdr : wdi) + (size_t)((cg < 7 ? cg : cg - 7) * 64 + wid * 8) * 896;
  const unsigned dofs = l3 * 896 + ((l7 ^ (l4 | ((wid & 3) << 1))) << 4);
  auto stage = [&](int st, int slot) {
    const unsigned l = ldsb + slot * 32768;
    glds_asm(xg + st * 128, xo0, l + wid * 2048);
    glds_asm(xg + st * 128, xo1, l + wid * 2048 + 1024);
    glds_asm(wg + (size_t)st * 64 * 3584, wo, l + 16384 + wid * 1024);
    glds_asm(wg + (size_t)st * 64 * 3584 + 1792, wo, l + 24576 + wid * 1024);
  };
  auto stage_d = [&](int c) { glds_asm(dgp + c * 128, dofs, ldsb + 65536 + c * 8192 + wid * 1024); };
#pragma unroll
  for (int s = 0; s < 5; ++s) stage(s, s);
  STAMP(10);
  const int grp = rp * 4 + w;
  const u64 tag = (*(const u64*)(ws + OFF_E + grp * 128) + EPOCH_STEP);
  const int fi = lane & 15, fq = lane >> 4;
  const unsigned arow = 8 * (fi >> 2) + (fi & 3);
  const unsigned ax0 = (32 * w + arow) * 128 + ((fq ^ (fi >> 1)) << 4);
  const unsigned ax1 = (32 * w + arow) * 128 + (((fq + 4) ^ (fi >> 1)) << 4);
  const unsigned tq = fi >> 2, tp = lane & 3, fw = ((tq >> 1) & 1) | ((fq & 1) << 1);
  unsigned bw[4];
#pragma unroll
  for (int n = 0; n < 4; ++n) bw[n] = (half ? 16384 : 24576) + (8 * fq + tq) * 128 + ((n ^ fw) << 5) + tp * 8;
  bf16x8 xf[14][2];
  f32x4 az[2][4];
#pragma unroll
  for (int h = 0; h < 2; ++h)
#pragma unroll
    for (int n = 0; n < 4; ++n) az[h][n] = (f32x4){0, 0, 0, 0};
#pragma unroll
  for (int t = 0; t < 7; ++t) {
    if (t == 0) WAITV(16); else if (t < 5) WAITV(12); else if (t == 5) WAITV(11); else WAITV(7);
    WAITL0();
    BAR();
    if (t == 0) STAMP(1);
    if (t == 5) STAMP(2);
    if (t == 1 || t == 2) stage(t + 4, t - 1);
    if (t == 3) { stage_d(0); stage_d(1); stage_d(2); stage_d(3); }
    if (t == 4) { stage_d(4); stage_d(5); stage_d(6); }
    LAS char* l = lds + (t % 5) * 32768;
#pragma unroll
    for (int k = 0; k < 2; ++k) {
#pragma unroll
      for (int h = 0; h < 2; ++h) xf[2 * t + k][h] = *(const LAS bf16x8*)(l + (k ? ax1 : ax0) + h * 512);
      bf16x8 wf[4];
#pragma unroll
      for (int n = 0; n < 4; ++n) {
        bf16x4 lo = __builtin_amdgcn_ds_read_tr16_b64_v4i16((LAS bf16x4*)(l + bw[n] + k * 4096));
        bf16x4 hi = __builtin_amdgcn_ds_read_tr16_b64_v4i16((LAS bf16x4*)(l + bw[n] + k * 4096 + 512));
        wf[n] = __builtin_shufflevector(lo, hi, 0, 1, 2, 3, 4, 5, 6, 7);
      }
#pragma unroll
      for (int n = 0; n < 4; ++n)
#pragma unroll
        for (int h = 0; h < 2; ++h) az[h][n] = MFMA(wf[n], xf[2 * t + k][h], az[h][n]);
    }
  }
  STAMP(3);
  WAITV(0);
  WAITL0();
  BAR();
  STAMP(4);
  auto pass2 = [&]() {
    f32x4 ab[2][2];
#pragma unroll
    for (int h = 0; h < 2; ++h)
#pragma unroll
      for (int n = 0; n < 2; ++n) ab[h][n] = (f32x4){0, 0, 0, 0};
    const unsigned d0 = 65536 + (32 * half + arow) * 128 + ((fq ^ (fi >> 1)) << 4);
    const unsigned d1 = 65536 + (32 * half + arow) * 128 + (((fq + 4) ^ (fi >> 1)) << 4);
#pragma unroll
    for (int ks = 0; ks < 14; ++ks) {
      bf16x8 df[2];
#pragma unroll
      for (int n = 0; n < 2; ++n) df[n] = *(const LAS bf16x8*)(lds + ((ks & 1) ? d1 : d0) + (ks >> 1) * 8192 + n * 512);
#pragma unroll
      for (int n = 0; n < 2; ++n)
#pragma unroll
        for (int h = 0; h < 2; ++h) ab[h][n] = MFMA(xf[ks][h], df[n], ab[h][n]);
    }
#pragma unroll
    for (int n = 0; n < 2; ++n) {
      const int T = 4 * cg + 2 * half + n, p = T / 7, n7 = T % 7;
      u32x4 o = {pk_bf16(ab[0][n][0], ab[0][n][1]), pk_bf16(ab[0][n][2], ab[0][n][3]),
                 pk_bf16(ab[1][n][0], ab[1][n][1]), pk_bf16(ab[1][n][2], ab[1][n][3])};
      *(u32x4*)(ws + OFF_BT + ((size_t)(p * 64 + 4 * rp + w) * 7 + n7) * 1024 + lane * 16) = o;
    }
  };
  auto do_gelu = [&]() {
#pragma unroll
    for (int h = 0; h < 2; ++h)
#pragma unroll
      for (int n = 0; n < 4; ++n)
#pragma unroll
        for (int j = 0; j < 4; ++j) az[h][n][j] = gelu(az[h][n][j]);
  };
  if (half) {
    pass2();
    STAMP(5);
    do_gelu();
    STAMP(6);
#pragma unroll
    for (int h = 0; h < 2; ++h)
#pragma unroll
      for (int k = 0; k < 2; ++k) {
        u32x4 o = {pk_bf16(az[h][2 * k][0], az[h][2 * k][1]), pk_bf16(az[h][2 * k][2], az[h][2 * k][3]),
                   pk_bf16(az[h][2 * k + 1][0], az[h][2 * k + 1][1]), pk_bf16(az[h][2 * k + 1][2], az[h][2 * k + 1][3])};
        *(u32x4*)(ws + OFF_Z1 + ((size_t)(8 * rp + 2 * w + h) * 28 + 2 * cg + k) * 1024 + lane * 16) = o;
      }
    STAMP_FLUSH(0);
    return;
  }
  do_gelu();
  STAMP(5);
  float mean_p[2], m2_p[2];
#pragma unroll
  for (int h = 0; h < 2; ++h) {
    float s = 0.f;
#pragma unroll
    for (int n = 0; n < 4; ++n) s += (az[h][n][0] + az[h][n][1]) + (az[h][n][2] + az[h][n][3]);
    s += __shfl_xor(s, 16);
    s += __shfl_xor(s, 32);
    mean_p[h] = s * (1.0f / 64.0f);
  }
#pragma unroll
  for (int h = 0; h < 2; ++h) {
    float q = 0.f;
#pragma unroll
    for (int n = 0; n < 4; ++n)
#pragma unroll
      for (int j = 0; j < 4; ++j) { float d = az[h][n][j] - mean_p[h]; q = __builtin_fmaf(d, d, q); }
    q += __shfl_xor(q, 16);
    q += __shfl_xor(q, 32);
    m2_p[h] = q;
  }
  GAS u64* xch = (GAS u64*)(ws + OFF_X) + (size_t)grp * 14 * 64 + lane;
  const u64 tag40 = tag << 24;
  {
    const float val = (fq & 2) ? ((fq & 1) ? m2_p[1] : m2_p[0]) : ((fq & 1) ? mean_p[1] : mean_p[0]);
    const unsigned bits = (__float_as_uint(val) + 0x80u) >> 8;
    __hip_atomic_store(xch + cg * 64, tag40 | bits, __ATOMIC_RELAXED, __HIP_MEMORY_SCOPE_AGENT);
  }
  STAMP(6);
  pass2();
  STAMP(7);
  float pv[14];
  for (unsigned spins = 0;; ++spins) {
    bool ok = true;
#pragma unroll
    for (int c = 0; c < 14; ++c) {
      const u64 g = __hip_atomic_load(xch + c * 64, __ATOMIC_RELAXED, __HIP_MEMORY_SCOPE_AGENT);
      pv[c] = __uint_as_float((unsigned)g << 8);
      ok &= ((g ^ tag40) >> 24) == 0;
    }
    if (__all(ok) || spins > (1u << 22)) break;
    __builtin_amdgcn_s_sleep(1);
  }
  STAMP(8);
  if (cg == 0 && lane == 0) __hip_atomic_store((GAS u64*)(ws + OFF_E + grp * 128), tag, __ATOMIC_RELAXED, __HIP_MEMORY_SCOPE_AGENT);
  float sum = 0.f;
#pragma unroll
  for (int c = 0; c < 14; ++c) sum += pv[c];
  const float mean = sum * (1.0f / 14.0f);
  float dev = 0.f;
#pragma unroll
  for (int c = 0; c < 14; ++c) { float d = pv[c] - mean; dev = __builtin_fmaf(d, d, dev); }
  const float mine = (fq & 2) ? sum : dev * 64.0f;
  const float m2 = mine + __shfl_xor(mine, 32);
  const float rstd = __builtin_amdgcn_rsqf(m2 * (1.0f / 896.0f) + 1e-5f);
  float mu[2], rs[2];
#pragma unroll
  for (int h = 0; h < 2; ++h) {
    mu[h] = __shfl(mean, fi + 16 * h);
    rs[h] = __shfl(rstd, fi + 16 * h);
  }
  LAS char* sc = lds + 4 * 32768 + w * 4096;
#pragma unroll
  for (int h = 0; h < 2; ++h)
#pragma unroll
    for (int n = 0; n < 4; ++n) {
      u32x2 o = {pk_bf16((az[h][n][0] - mu[h]) * rs[h], (az[h][n][1] - mu[h]) * rs[h]),
                 pk_bf16((az[h][n][2] - mu[h]) * rs[h], (az[h][n][3] - mu[h]) * rs[h])};
      *(LAS u32x2*)(sc + (arow + 4 * h) * 128 + (((4 * n + fq) ^ fi) << 3)) = o;
    }
  WAITL0();
#pragma unroll
  for (int n = 0; n < 4; ++n) {
    const unsigned a = (8 * fq + tq) * 128 + (((4 * n + tp) ^ ((fq << 2) | tq)) << 3);
    bf16x4 lo = __builtin_amdgcn_ds_read_tr16_b64_v4i16((LAS bf16x4*)(sc + a));
    bf16x4 hi = __builtin_amdgcn_ds_read_tr16_b64_v4i16((LAS bf16x4*)(sc + a + 512));
    bf16x8 o = __builtin_shufflevector(lo, hi, 0, 1, 2, 3, 4, 5, 6, 7);
    const int T = 56 + 4 * cg + n, p = T / 7, n7 = T % 7;
    *(bf16x8*)(ws + OFF_BT + ((size_t)(p * 64 + 4 * rp + w) * 7 + n7) * 1024 + lane * 16) = o;
  }
  STAMP(9);
  STAMP_FLUSH(0);
}

constexpr int STA = 16384, STB = 14336, STG = STA + STB, RING = 5, NT = 32;

template <int P> __device__ __forceinline__ void waitv_tail(int n) {
  switch (n) {
    case 0: WAITV(0); break;
    case 1: WAITV(P); break;
    case 2: WAITV(2 * P); break;
    default: WAITV(3 * P); break;
  }
}

template <int ODD>
__device__ __forceinline__ void k2_loadz(const char* zp, u32x4 (&zq)[2][3], u32x2 (&zs)[2]) {
#pragma unroll
  for (int m = 0; m < 2; ++m) {
    const char* zm = zp + (size_t)m * 28 * 1024;
#pragma unroll
    for (int k = 0; k < 3; ++k) zq[m][k] = *(const u32x4*)(zm + (k + ODD) * 1024);
    zs[m] = *(const u32x2*)(zm + (ODD ? 8 : 3 * 1024));
  }
}
template <int ODD>
__device__ __forceinline__ void k2_epi(f32x4 (&acc)[2][7], bool gate, const float (&bb)[2], const u32x4 (&zq)[2][3],
                                       const u32x2 (&zs)[2], char* op) {
#pragma unroll
  for (int m = 0; m < 2; ++m) {
    if (gate) {
#pragma unroll
      for (int n = 0; n < 7; ++n) {
        const bool single = ODD ? (n == 0) : (n == 6);
        const int nn = n - ODD;
        const unsigned z0 = single ? zs[m][0] : zq[m][(nn < 0 ? 0 : nn) >> 1 < 3 ? (nn < 0 ? 0 : nn) >> 1 : 0][(nn & 1) * 2];
        const unsigned z1 = single ? zs[m][1] : zq[m][(nn < 0 ? 0 : nn) >> 1 < 3 ? (nn < 0 ? 0 : nn) >> 1 : 0][(nn & 1) * 2 + 1];
        acc[m][n][0] = (acc[m][n][0] + bb[m]) * bflo(z0);
        acc[m][n][1] = (acc[m][n][1] + bb[m]) * bfhi(z0);
        acc[m][n][2] = (acc[m][n][2] + bb[m]) * bflo(z1);
        acc[m][n][3] = (acc[m][n][3] + bb[m]) * bfhi(z1);
      }
    }
    char* om = op + (size_t)m * 56 * 1024;
#pragma unroll
    for (int k = 0; k < 3; ++k) {
      const f32x4 a = acc[m][2 * k + ODD], c = acc[m][2 * k + 1 + ODD];
      u32x4 o = {pk_bf16(a[0], a[1]), pk_bf16(a[2], a[3]), pk_bf16(c[0], c[1]), pk_bf16(c[2], c[3])};
      *(u32x4*)(om + (k + ODD) * 1024) = o;
    }
    const f32x4 a = acc[m][ODD ? 0 : 6];
    u32x2 o = {pk_bf16(a[0], a[1]), pk_bf16(a[2], a[3])};
    *(u32x2*)(om + (ODD ? 8 : 3 * 1024)) = o;
  }
}

__global__ __launch_bounds__(512, 2) void k2(const u16* __restrict__ wsr, const u16* __restrict__ wsi, const u16* __restrict__ wsp,
                                             const u16* __restrict__ bsp, char* __restrict__ ws) {
  __shared__ __attribute__((aligned(1024))) char smem[RING * STG];
  LAS char* lds = (LAS char*)smem;
  const int wid = __builtin_amdgcn_readfirstlane(threadIdx.x >> 6), lane = threadIdx.x & 63;
  STAMP_DECL; STAMP(0);
  const int b = blockIdx.x, xcd = b & 7, r = b >> 3;
  int mi, p;
  if (xcd < 4) { mi = xcd * 4 + (r & 3); p = 8 + (r >> 2); }
  else { const int h = xcd - 4; mi = (h & 1) * 8 + (r & 7); p = (h >> 1) * 4 + (r >> 3); }
  if (wid >= 4) {
    const int lw = wid - 4;
    if (lw < 2) {
      const u16* A = p < 4 ? wsr : (p < 8 ? wsi : wsp);
      const char* base = (const char*)A + (size_t)(mi * 128 + lw * 64) * 4096;
      unsigned off[4];
#pragma unroll
      for (int i = 0; i < 4; ++i) off[i] = (unsigned)((lane >> 3) * 4096 + i * 32768 + (((lane & 7) ^ (((lane >> 4) & 1) | (i << 1))) << 4));
      auto issue = [&](int st, int slot) {
        const char* g = base + st * 128;
        LAS char* l = lds + slot * STG + lw * 8192;
#pragma unroll
        for (int i = 0; i < 8; ++i) GLDS(g + (size_t)(i >> 2) * 131072 + off[i & 3], l + i * 1024, AUX_A);
      };
#pragma unroll
      for (int s = 0; s < RING; ++s) issue(s, s);
      STAMP(1);
      WAITV(8 * RING - 1);
      STAMP(2);
      WAITV(8 * (RING - 1));
      STAMP(3);
      BAR();
      int slot = 0;
      for (int t = 0; t < NT - RING; ++t) {
        WAITV(8 * (RING - 2));
        BAR();
        issue(t + RING, slot);
        slot = (slot == RING - 1) ? 0 : slot + 1;
      }
#pragma unroll
      for (int t = NT - RING; t < NT - 1; ++t) { waitv_tail<8>(NT - 2 - t); BAR(); }
    } else {
      const char* base = ws + OFF_BT + (size_t)p * 64 * 7 * 1024 + (lw - 2) * 7168;
      const unsigned off = lane * 16;
      auto issue = [&](int st, int slot) {
        const char* g = base + (size_t)st * STB;
        LAS char* l = lds + slot * STG + STA + (lw - 2) * 7168;
#pragma unroll
        for (int i = 0; i < 7; ++i) GLDS(g + i * 1024 + off, l + i * 1024, 0);
      };
#pragma unroll
      for (int s = 0; s < RING; ++s) issue(s, s);
      STAMP(4);
      WAITV(7 * RING - 1);
      STAMP(5);
      WAITV(7 * (RING - 1));
      STAMP(6);
      BAR();
      int slot = 0;
      for (int t = 0; t < NT - RING; ++t) {
        WAITV(7 * (RING - 2));
        BAR();
        issue(t + RING, slot);
        slot = (slot == RING - 1) ? 0 : slot + 1;
      }
#pragma unroll
      for (int t = NT - RING; t < NT - 1; ++t) { waitv_tail<7>(NT - 2 - t); BAR(); }
    }
    STAMP_FLUSH(1);
    return;
  }
  const int fi = lane & 15, fq = lane >> 4;
  const unsigned arow = 8 * (fi >> 2) + (fi & 3);
  const unsigned aoff0 = (32 * wid + arow) * 128 + ((fq ^ (fi >> 1)) << 4);
  const unsigned aoff1 = (32 * wid + arow) * 128 + (((fq + 4) ^ (fi >> 1)) << 4);
  const unsigned boff = STA + lane * 16;
  const int mt0 = mi * 8 + wid * 2;
  const bool gate = p >= 8;
  u32x4 zq[2][3];
  u32x2 zs[2];
  float bb[2] = {0.f, 0.f};
  if (gate) {
    const char* zp = ws + OFF_Z1 + ((size_t)mt0 * 28 + ((7 * (p - 8)) >> 1)) * 1024 + lane * 16;
    if (p & 1) k2_loadz<1>(zp, zq, zs); else k2_loadz<0>(zp, zq, zs);
    bb[0] = bf2f(bsp[mi * 128 + 32 * wid + arow]);
    bb[1] = bf2f(bsp[mi * 128 + 32 * wid + arow + 4]);
  }
  f32x4 acc[2][7];
#pragma unroll
  for (int m = 0; m < 2; ++m)
#pragma unroll
    for (int n = 0; n < 7; ++n) acc[m][n] = (f32x4){0, 0, 0, 0};
  bf16x8 fa[2][2][2], fb[2][2][7];
  auto rd = [&](int set, int slot) {
    LAS char* l = lds + slot * STG;
#pragma unroll
    for (int k = 0; k < 2; ++k) {
#pragma unroll
      for (int m = 0; m < 2; ++m) fa[set][k][m] = *(const LAS bf16x8*)(l + (k ? aoff1 : aoff0) + m * 512);
#pragma unroll
      for (int n = 0; n < 7; ++n) fb[set][k][n] = *(const LAS bf16x8*)(l + boff + k * 7168 + n * 1024);
    }
  };
  auto mm = [&](int set) {
#pragma unroll
    for (int k = 0; k < 2; ++k)
#pragma unroll
      for (int n = 0; n < 7; ++n)
#pragma unroll
        for (int m = 0; m < 2; ++m) acc[m][n] = MFMA(fb[set][k][n], fa[set][k][m], acc[m][n]);
  };
  auto sched = [&]() {
#pragma unroll
    for (int i = 0; i < 18; ++i) {
      __builtin_amdgcn_sched_group_barrier(0x8, 1, 0);
      __builtin_amdgcn_sched_group_barrier(0x100, 1, 0);
    }
    __builtin_amdgcn_sched_group_barrier(0x8, 10, 0);
    __builtin_amdgcn_sched_barrier(0);
  };
  BAR();
  STAMP(1);
  rd(0, 0);
  int slot = 1;
  for (int t = 0; t < NT - 2; t += 2) {
    WAITL0(); BAR();
    rd(1, slot); mm(0); sched();
    slot = (slot == RING - 1) ? 0 : slot + 1;
    WAITL0(); BAR();
    rd(0, slot); mm(1); sched();
    slot = (slot == RING - 1) ? 0 : slot + 1;
  }
  WAITL0(); BAR();
  rd(1, slot); mm(0); sched();
  mm(1);
  STAMP(2);
  char* op = ws + OFF_O2 + ((size_t)mt0 * 56 + ((7 * p) >> 1)) * 1024 + lane * 16;
  if (p & 1) k2_epi<1>(acc, gate, bb, zq, zs, op); else k2_epi<0>(acc, gate, bb, zq, zs, op);
  STAMP(3);
  STAMP_FLUSH(1);
}

__global__ __launch_bounds__(512, 2) void k3(const char* __restrict__ ws, u16* __restrict__ out) {
  __shared__ __attribute__((aligned(1024))) char smem[112 * 1024];
  LAS char* lds = (LAS char*)smem;
  const int wid = __builtin_amdgcn_readfirstlane(threadIdx.x >> 6), lane = threadIdx.x & 63;
  STAMP_DECL; STAMP(0);
  const int b = blockIdx.x, xcd = b & 7, r = b >> 3;
  const int rg = 4 * xcd + (r & 3), dg = r >> 2;
  if (wid >= 4) {
    const int lw = wid - 4;
    const char* g = ws + OFF_WO + (size_t)dg * 112 * 1024 + lw * 1024 + lane * 16;
#pragma unroll
    for (int kb = 0; kb < 28; ++kb) GLDS(g + kb * 4096, lds + kb * 4096 + lw * 1024, 0);
    STAMP(1);
    WAITV(27); STAMP(2);
    WAITV(24); STAMP(3); BAR();
    WAITV(20); STAMP(4); BAR();
    WAITV(16); STAMP(5); BAR();
    WAITV(12); STAMP(6); BAR();
    WAITV(8); STAMP(7); BAR();
    WAITV(4); STAMP(8); BAR();
    WAITV(0); STAMP(9); BAR();
    STAMP_FLUSH(2);
    return;
  }
  const int mt = 4 * rg + wid, fi = lane & 15, fq = lane >> 4;
  const char* o2 = ws + OFF_O2 + (size_t)mt * 56 * 1024 + lane * 16;
  bf16x8 hf[28];
#pragma unroll
  for (int kb = 0; kb < 28; ++kb) hf[kb] = *(const bf16x8*)(o2 + (28 + kb) * 1024);
  u32x4 yr[2], yi[2];
#pragma unroll
  for (int k = 0; k < 2; ++k) {
    yr[k] = *(const u32x4*)(o2 + (2 * dg + k) * 1024);
    yi[k] = *(const u32x4*)(o2 + (14 + 2 * dg + k) * 1024);
  }
  const int t = 32 * (mt >> 1) + 8 * (fi >> 2) + 4 * (mt & 1) + (fi & 3);
  const float g = ((const float*)(ws + OFF_G))[t];
  __builtin_amdgcn_sched_barrier(0);
  STAMP(5);
  f32x4 acc[4];
#pragma unroll
  for (int n = 0; n < 4; ++n) acc[n] = (f32x4){0, 0, 0, 0};
#pragma unroll
  for (int c = 0; c < 7; ++c) {
    BAR();
    if (c == 0) STAMP(1);
    if (c == 6) STAMP(2);
#pragma unroll
    for (int k = 0; k < 4; ++k) {
      const int kb = 4 * c + k;
#pragma unroll
      for (int n = 0; n < 4; ++n) {
        const bf16x8 wf = *(const LAS bf16x8*)(lds + kb * 4096 + n * 1024 + lane * 16);
        acc[n] = MFMA(wf, hf[kb], acc[n]);
      }
    }
  }
  STAMP(3);
  const float g1 = 1.0f - g;
#pragma unroll
  for (int k = 0; k < 2; ++k) {
    float v[8];
#pragma unroll
    for (int e = 0; e < 4; ++e) {
      const float a0 = acc[2 * k + (e >> 1)][(e & 1) * 2], a1 = acc[2 * k + (e >> 1)][(e & 1) * 2 + 1];
      v[2 * e] = g * (bflo(yr[k][e]) - bflo(yi[k][e])) + g1 * a0;
      v[2 * e + 1] = g * (bfhi(yr[k][e]) - bfhi(yi[k][e])) + g1 * a1;
    }
    u32x4 o = {pk_bf16(v[0], v[1]), pk_bf16(v[2], v[3]), pk_bf16(v[4], v[5]), pk_bf16(v[6], v[7])};
    *(u32x4*)(out + (size_t)t * D + 64 * dg + 32 * k + 8 * fq) = o;
  }
  STAMP(4);
  STAMP_FLUSH(2);
}

template <int AUX>
__global__ __launch_bounds__(256) void k0(const char* __restrict__ a0, const char* __restrict__ a1, const char* __restrict__ a2) {
  __shared__ __attribute__((aligned(1024))) char smem[4 * 16 * 1024];
  LAS char* lds = (LAS char*)smem;
  const int wid = __builtin_amdgcn_readfirstlane(threadIdx.x >> 6), lane = threadIdx.x & 63;
  const size_t off = (size_t)blockIdx.x * 32768 + wid * 8192 + lane * 16;
#pragma unroll
  for (int i = 0; i < 8; ++i) GLDS(a0 + off + i * 1024, lds + wid * 16384 + i * 1024, AUX);
#pragma unroll
  for (int i = 0; i < 8; ++i) GLDS(a1 + off + i * 1024, lds + wid * 16384 + 8192 + i * 1024, AUX);
#pragma unroll
  for (int i = 0; i < 8; ++i) GLDS(a2 + off + i * 1024, lds + wid * 16384 + i * 1024, AUX);
  WAITV(0);
}
extern "C" void kernel_launch(void* const* d_in, const int* in_sizes, int n_in, void* d_out, int out_size, void* d_ws,
                              size_t ws_size, hipStream_t stream) {
  const u16* x = (const u16*)d_in[0];
  const u16* wsr = (const u16*)d_in[1];
  const u16* wsi = (const u16*)d_in[2];
  const u16* wdr = (const u16*)d_in[3];
  const u16* wdi = (const u16*)d_in[4];
  const u16* win = (const u16*)d_in[6];
  const u16* wsp = (const u16*)d_in[7];
  const u16* bsp = (const u16*)d_in[8];
  const u16* wout = (const u16*)d_in[9];
  const u16* gw = (const u16*)d_in[10];
  char* ws = (char*)d_ws;
  k1<<<256, 512, 0, stream>>>(x, wdr, wdi, win, wout, gw, ws);
  k2<<<256, 512, 0, stream>>>(wsr, wsi, wsp, bsp, ws);
  k3<<<224, 512, 0, stream>>>(ws, (u16*)d_out);
}
```

```cpp
#include <hip/hip_runtime.h>
#include <hip/hip_bf16.h>

typedef unsigned short u16;
typedef unsigned long long u64;
typedef short bf16x8 __attribute__((ext_vector_type(8)));
typedef short bf16x4 __attribute__((ext_vector_type(4)));
typedef float f32x4 __attribute__((ext_vector_type(4)));
typedef unsigned u32x2 __attribute__((ext_vector_type(2)));
typedef unsigned u32x4 __attribute__((ext_vector_type(4)));
#define LAS __attribute__((address_space(3)))
#define GAS __attribute__((address_space(1)))

#define WAITV(n) asm volatile("s_waitcnt vmcnt(%0)" ::"n"(n) : "memory")
#define WAITL0() asm volatile("s_waitcnt lgkmcnt(0)" ::: "memory")
#define BAR() do { asm volatile("" ::: "memory"); __builtin_amdgcn_s_barrier(); asm volatile("" ::: "memory"); } while (0)
#define GLDS(g, l, aux) __builtin_amdgcn_global_load_lds((const GAS void*)(g), (LAS void*)(l), 16, 0, aux)
__device__ __forceinline__ void glds_asm(const void* sbase, unsigned voff, unsigned l) {
  unsigned keep;
  l = __builtin_amdgcn_readfirstlane(l);
  asm volatile("s_mov_b32 %0, m0\n\ts_mov_b32 m0, %3\n\ts_nop 0\n\tglobal_load_lds_dwordx4 %1, %2\n\ts_mov_b32 m0, %0"
               : "=&s"(keep) : "v"(voff), "s"(sbase), "s"(l) : "memory");
}
__device__ __forceinline__ void touch(const void* sbase, unsigned voff, unsigned l) {
  unsigned keep;
  asm volatile("s_mov_b32 %0, m0\n\ts_mov_b32 m0, %3\n\ts_nop 0\n\tglobal_load_lds_dword %1, %2\n\ts_mov_b32 m0, %0"
               : "=&s"(keep) : "v"(voff), "s"(sbase), "s"(l) : "memory");
}
#define ST_AUX 0
template <class T> __device__ __forceinline__ void st16(void* p, T v) {
  static_assert(sizeof(T) == 16, "");
  *(T*)p = v;
}
__device__ __forceinline__ void st8(void* p, u32x2 v) {
  *(u32x2*)p = v;
}
#define PW1 4
#define PW2 0
#define MFMA(a, b, c) __builtin_amdgcn_mfma_f32_16x16x32_bf16(a, b, c, 0, 0, 0)

#define AUX_A 0

constexpr int S = 2048, D = 448, F = 1792, FH = 896;

__device__ __forceinline__ float bf2f(u16 v) { return __uint_as_float(((unsigned)v) << 16); }
__device__ __forceinline__ float bflo(unsigned v) { return __uint_as_float(v << 16); }
__device__ __forceinline__ float bfhi(unsigned v) { return __uint_as_float(v & 0xffff0000u); }
__device__ __forceinline__ unsigned pk_bf16(float lo, float hi) {
  typedef __bf16 bf2 __attribute__((ext_vector_type(2)));
  typedef float f2 __attribute__((ext_vector_type(2)));
  f2 v = {lo, hi};
  bf2 b = __builtin_convertvector(v, bf2);
  return *(unsigned*)&b;
}
__device__ __forceinline__ float gelu(float v) {
  const float c0 = -2.0f * 0.7978845608028654f * 1.4426950408889634f;
  const float c1 = c0 * 0.044715f;
  float e = __builtin_amdgcn_exp2f(v * __builtin_fmaf(v * v, c1, c0));
  return v * __builtin_amdgcn_rcpf(1.0f + e);
}

typedef float f32x2 __attribute__((ext_vector_type(2)));
__device__ __forceinline__ f32x2 gelu2(f32x2 v) {
  const float c0 = -2.0f * 0.7978845608028654f * 1.4426950408889634f;
  const float c1 = c0 * 0.044715f;
  const f32x2 u = v * ((v * v) * c1 + c0);
  f32x2 e = {__builtin_amdgcn_exp2f(u.x), __builtin_amdgcn_exp2f(u.y)};
  e = e + 1.0f;
  const f32x2 rc = {__builtin_amdgcn_rcpf(e.x), __builtin_amdgcn_rcpf(e.y)};
  return v * rc;
}

__device__ __forceinline__ float xsum(float s) {
  auto a = __builtin_amdgcn_permlane16_swap(__float_as_uint(s), __float_as_uint(s), false, false);
  s = __uint_as_float(a[0]) + __uint_as_float(a[1]);
  auto b = __builtin_amdgcn_permlane32_swap(__float_as_uint(s), __float_as_uint(s), false, false);
  return __uint_as_float(b[0]) + __uint_as_float(b[1]);
}
#define K1_ABL 0
#define K1_ORDER 1

constexpr size_t OFF_BT = 0;
constexpr size_t OFF_O2 = OFF_BT + (size_t)1792 * S * 2;
constexpr size_t OFF_Z1 = OFF_O2 + (size_t)1792 * S * 2;
constexpr size_t OFF_WO = OFF_Z1 + (size_t)896 * S * 2;
constexpr size_t OFF_G = OFF_WO + (size_t)896 * 448 * 2;
constexpr size_t OFF_X = OFF_G + 8192;
constexpr size_t OFF_E = OFF_X + (size_t)64 * 14 * 512;
#define STAMP_DECL
#define STAMP(i)
#define STAMP_FLUSH(k)
constexpr u64 EPOCH_STEP = 0x9E3779B97F4A7C15ull;

__global__ __launch_bounds__(512, 2) void k1(const u16* __restrict__ x, const u16* __restrict__ wdr, const u16* __restrict__ wdi,
                                             const u16* __restrict__ win, const u16* __restrict__ wout, const u16* __restrict__ gw,
                                             const u16* __restrict__ wsr, const u16* __restrict__ wsi, const u16* __restrict__ wsp,
                                             char* __restrict__ ws) {
  __shared__ __attribute__((aligned(1024))) char smem[5 * 32768];
  LAS char* lds = (LAS char*)smem;
  const int wid = __builtin_amdgcn_readfirstlane(threadIdx.x >> 6), lane = threadIdx.x & 63;
  STAMP_DECL; STAMP(0);
  const int b = blockIdx.x, xcd = b & 7, r = b >> 3;
  if (r >= 28) {
    const int sj = xcd * 4 + (r - 28);
    {
      u32x4 gv = {0, 0, 0, 0}, xv[8];
      if (lane < 56) gv = *(const u32x4*)(gw + lane * 8);
      const int row0 = sj * 64 + wid * 8;
#pragma unroll
      for (int i = 0; i < 8; ++i) {
        xv[i] = (u32x4){0, 0, 0, 0};
        if (lane < 56) xv[i] = *(const u32x4*)(x + (size_t)(row0 + i) * D + lane * 8);
      }
      float res = 0.f;
#pragma unroll
      for (int i = 0; i < 8; ++i) {
        float a = 0.f;
#pragma unroll
        for (int j = 0; j < 4; ++j) a += bflo(xv[i][j]) * bflo(gv[j]) + bfhi(xv[i][j]) * bfhi(gv[j]);
#pragma unroll
        for (int o = 32; o >= 1; o >>= 1) a += __shfl_xor(a, o);
        if (lane == i) res = a;
      }
      if (lane < 8) ((float*)(ws + OFF_G))[row0 + lane] = 1.0f / (1.0f + __expf(-res));
    }
    STAMP(1);
    const int rr = lane & 15, q = lane >> 4;
    u16 v[4][8];
#pragma unroll
    for (int it = 0; it < 4; ++it) {
      int blk = sj * 8 + wid + it * 256;
      if (blk >= 784) blk = 783;
      const int t4 = blk & 3, kb = (blk >> 2) % 28, dg = (blk >> 2) / 28;
      const int d = 64 * dg + 32 * (t4 >> 1) + 8 * (rr >> 2) + 4 * (t4 & 1) + (rr & 3);
#pragma unroll
      for (int e = 0; e < 8; ++e) v[it][e] = wout[(size_t)(32 * kb + 16 * (e >> 2) + 4 * q + (e & 3)) * D + d];
    }
#pragma unroll
    for (int it = 0; it < 4; ++it) {
      const int blk = sj * 8 + wid + it * 256;
      u32x4 o = {v[it][0] | ((unsigned)v[it][1] << 16), v[it][2] | ((unsigned)v[it][3] << 16),
                 v[it][4] | ((unsigned)v[it][5] << 16), v[it][6] | ((unsigned)v[it][7] << 16)};
      if (blk < 784) st16(ws + OFF_WO + (size_t)blk * 1024 + lane * 16, o);
    }
    STAMP_FLUSH(0);
    return;
  }
  const int rp = 4 * (xcd >> 1) + (r & 3), cg = 7 * (xcd & 1) + (r >> 2);
  const int w = wid & 3, half = wid >> 2;
  constexpr unsigned WD0 = 98304, JUNK = WD0 + 57344;
  const unsigned ldsb = __builtin_amdgcn_readfirstlane((unsigned)(size_t)lds);
  const unsigned l3 = lane >> 3, l7 = lane & 7, l4 = (lane >> 4) & 1;
  const char* xg = (const char*)x + (size_t)(rp * 128 + wid * 16) * 896;
  const unsigned xo0 = l3 * 896 + ((l7 ^ (l4 | (((2 * wid) & 3) << 1))) << 4);
  const unsigned xo1 = (l3 + 8) * 896 + ((l7 ^ (l4 | (((2 * wid + 1) & 3) << 1))) << 4);
  const char* wg = (const char*)win + (size_t)(wid * 8) * 3584 + cg * 128;
  const unsigned wo = l3 * 3584 + (((((l7 >> 1) ^ (l4 | ((wid & 1) << 1))) << 1) | (l7 & 1)) << 4);
  const int cls = ((r >> 2) + r) & 3;
  const unsigned ord = cls == 0 ? 0x6543120u : cls == 1 ? 0x6532410u : cls == 2 ? 0x6542031u : 0x6423501u;
  auto piece = [&](int t, int i) {
    const unsigned st = (ord >> (4 * t)) & 7;
    const unsigned l = ldsb + (t % 3) * 32768;
    if (i == 0) glds_asm(xg + st * 128, xo0, l + wid * 2048);
    if (i == 1) glds_asm(xg + st * 128, xo1, l + wid * 2048 + 1024);
    if (i == 2) glds_asm(wg + st * (64 * 3584) + 1792, wo, l + 24576 + wid * 1024);
    if (i == 3) glds_asm(wg + st * (64 * 3584), wo, l + 16384 + wid * 1024);
  };
#pragma unroll
  for (int t = 0; t < 2; ++t)
#pragma unroll
    for (int i = 0; i < 4; ++i) piece(t, i);
  const char* dgp = (const char*)(cg < 7 ? wdr : wdi) + (size_t)((cg < 7 ? cg : cg - 7) * 64 + wid * 8) * 896;
  const unsigned dofs = l3 * 896 + ((l7 ^ (l4 | ((wid & 3) << 1))) << 4);
  auto piece_d = [&](int c) { glds_asm(dgp + ((ord >> (4 * c)) & 7) * 128, dofs, ldsb + WD0 + c * 8192 + wid * 1024); };
  const u16* pw0 = xcd < 6 ? wsr : wsi;
  pw0 = xcd < 4 ? wsp : pw0;
  const char* A = (const char*)pw0 + (size_t)(xcd < 4 ? xcd * 512 : (xcd & 1) * 1024) * 4096;
  const int lg = xcd < 4 ? 9 : 10;
  auto prewarm = [=](int j) {
    const unsigned i = (unsigned)(j * 28 + r) * 64 + lane;
    const unsigned row = (i >> 2) & ((1u << lg) - 1), blk = (i >> (lg + 2)) & 7;
    touch(A, row * 4096 + blk * 512 + (i & 3) * 128, ldsb + JUNK);
  };
  STAMP(10);
  const int grp = rp * 4 + w;
  u64 tag = *(const __attribute__((address_space(4))) u64*)(ws + OFF_E + grp * 128);
  asm volatile("" : "+s"(tag));
  tag += EPOCH_STEP;
  const int fi = lane & 15, fq = lane >> 4;
  const unsigned arow = 8 * (fi >> 2) + (fi & 3);
  unsigned ax[2];
  ax[0] = (32 * w + arow) * 128 + ((fq ^ (fi >> 1)) << 4);
  ax[1] = (32 * w + arow) * 128 + (((fq + 4) ^ (fi >> 1)) << 4);
  const unsigned tq = fi >> 2, tp = lane & 3, fw = ((tq >> 1) & 1) | ((fq & 1) << 1);
  unsigned bw[4];
#pragma unroll
  for (int n = 0; n < 4; ++n) bw[n] = (half ? 16384 : 24576) + (8 * fq + tq) * 128 + ((n ^ fw) << 5) + tp * 8;
  bf16x8 xf[14][2];
  bf16x4 wf[2][2][4][2];
  f32x4 az[2][4];
#pragma unroll
  for (int h = 0; h < 2; ++h)
#pragma unroll
    for (int n = 0; n < 4; ++n) { az[h][n] = (f32x4){0, 0, 0, 0}; asm volatile("" : "+v"(az[h][n])); }
  auto rdg = [&](int t, int g) {
    LAS char* l = lds + (t % 3) * 32768;
    const int k = g >> 1, h = g & 1;
    xf[2 * t + k][h] = *(const LAS bf16x8*)(l + ax[k] + h * 512);
#pragma unroll
    for (int j = 0; j < 2; ++j) {
      const int n = 2 * h + j;
      wf[t & 1][k][n][0] = __builtin_amdgcn_ds_read_tr16_b64_v4i16((LAS bf16x4*)(l + bw[n] + k * 4096));
      wf[t & 1][k][n][1] = __builtin_amdgcn_ds_read_tr16_b64_v4i16((LAS bf16x4*)(l + bw[n] + k * 4096 + 512));
    }
  };
  auto mmg = [&](int t, int g, int i) {
    const int k = g >> 1, n = 2 * (g & 1) + (i >> 1), h = i & 1;
    const bf16x8 wv = __builtin_shufflevector(wf[t & 1][k][n][0], wf[t & 1][k][n][1], 0, 1, 2, 3, 4, 5, 6, 7);
    az[h][n] = MFMA(wv, xf[2 * t + k][h], az[h][n]);
  };
  WAITV(4);
  BAR();
  STAMP(1);
#pragma unroll
  for (int g = 0; g < 4; ++g) rdg(0, g);
#pragma unroll
  for (int i = 0; i < 4; ++i) piece(2, i);
#pragma unroll
  for (int t = 0; t < 6; ++t) {
    __builtin_amdgcn_sched_barrier(0);
    WAITV(4);
    WAITL0();
    BAR();
    if (t == 4) STAMP(2);
#pragma unroll
    for (int g = 0; g < 4; ++g) {
      mmg(t, g, 0);
      __builtin_amdgcn_sched_barrier(0);
      if (t < 4) piece(t + 3, g);
      if (t == 4) piece_d(g);
      if (t == 5 && g < 3) piece_d(4 + g);
      rdg(t + 1, g);
      mmg(t, g, 1);
      mmg(t, g, 2);
      mmg(t, g, 3);
      __builtin_amdgcn_sched_group_barrier(0x8, 1, 0);
      __builtin_amdgcn_sched_group_barrier(0x100, 2, 0);
      __builtin_amdgcn_sched_group_barrier(0x8, 1, 0);
      __builtin_amdgcn_sched_group_barrier(0x100, 2, 0);
      __builtin_amdgcn_sched_group_barrier(0x8, 1, 0);
      __builtin_amdgcn_sched_group_barrier(0x100, 1, 0);
      __builtin_amdgcn_sched_barrier(0);
    }
  }
#pragma unroll
  for (int g = 0; g < 4; ++g)
#pragma unroll
    for (int i = 0; i < 4; ++i) mmg(6, g, i);
  STAMP(3);
#pragma unroll
  for (int h = 0; h < 2; ++h)
#pragma unroll
    for (int n = 0; n < 4; ++n) {
      const f32x2 lo = gelu2((f32x2){az[h][n][0], az[h][n][1]}), hi = gelu2((f32x2){az[h][n][2], az[h][n][3]});
      az[h][n] = (f32x4){lo.x, lo.y, hi.x, hi.y};
      asm volatile("" : "+v"(az[h][n]));
    }
  STAMP(4);
  WAITV(0);
  BAR();
  if (wid < PW1) prewarm(wid);
  STAMP(5);
  f32x4 ab[2][2];
  bf16x8 df[14][2];
  const unsigned dofs0 = WD0 + (32 * half + arow) * 128 + ((fq ^ (fi >> 1)) << 4);
  const unsigned dofs1 = WD0 + (32 * half + arow) * 128 + (((fq + 4) ^ (fi >> 1)) << 4);
  auto rdd = [&](int ks) {
#pragma unroll
    for (int n = 0; n < 2; ++n) df[ks][n] = *(const LAS bf16x8*)(lds + ((ks & 1) ? dofs1 : dofs0) + (ks >> 1) * 8192 + n * 512);
  };
#pragma unroll
  for (int ks = 0; ks < 4; ++ks) rdd(ks);
  __builtin_amdgcn_sched_barrier(0);
  auto pass2 = [&]() {
#pragma unroll
    for (int h = 0; h < 2; ++h)
#pragma unroll
      for (int n = 0; n < 2; ++n) ab[h][n] = (f32x4){0, 0, 0, 0};
    __builtin_amdgcn_sched_barrier(0);
#pragma unroll
    for (int ks = 0; ks < 14; ++ks) {
      if (ks + 4 < 14) rdd(ks + 4);
#pragma unroll
      for (int n = 0; n < 2; ++n)
#pragma unroll
        for (int h = 0; h < 2; ++h) ab[h][n] = MFMA(xf[ks][h], df[ks][n], ab[h][n]);
      if (ks + 4 < 14) {
        __builtin_amdgcn_sched_group_barrier(0x8, 1, 0);
        __builtin_amdgcn_sched_group_barrier(0x100, 1, 0);
        __builtin_amdgcn_sched_group_barrier(0x8, 1, 0);
        __builtin_amdgcn_sched_group_barrier(0x100, 1, 0);
        __builtin_amdgcn_sched_group_barrier(0x8, 2, 0);
      }
      __builtin_amdgcn_sched_barrier(0);
    }
  };
  auto store_ab = [&]() {
#pragma unroll
    for (int n = 0; n < 2; ++n) {
      const int T = 4 * cg + 2 * half + n, p = T / 7, n7 = T % 7;
      u32x4 o = {pk_bf16(ab[0][n][0], ab[0][n][1]), pk_bf16(ab[0][n][2], ab[0][n][3]),
                 pk_bf16(ab[1][n][0], ab[1][n][1]), pk_bf16(ab[1][n][2], ab[1][n][3])};
      st16(ws + OFF_BT + ((size_t)(p * 64 + 4 * rp + w) * 7 + n7) * 1024 + lane * 16, o);
    }
  };
  GAS u64* xch = (GAS u64*)(ws + OFF_X) + (size_t)grp * 14 * 64 + lane;
  const u64 tag40 = tag << 24;
  if (!half) {
    float mean_p[2], m2_p[2];
#pragma unroll
    for (int h = 0; h < 2; ++h) {
      const f32x4 s4 = (az[h][0] + az[h][1]) + (az[h][2] + az[h][3]);
      mean_p[h] = xsum((s4[0] + s4[1]) + (s4[2] + s4[3])) * (1.0f / 64.0f);
    }
#pragma unroll
    for (int h = 0; h < 2; ++h) {
      f32x4 q4 = {0, 0, 0, 0};
#pragma unroll
      for (int n = 0; n < 4; ++n) { const f32x4 d = az[h][n] - mean_p[h]; q4 += d * d; }
      m2_p[h] = xsum((q4[0] + q4[1]) + (q4[2] + q4[3]));
    }
    const float val = (fq & 2) ? ((fq & 1) ? m2_p[1] : m2_p[0]) : ((fq & 1) ? mean_p[1] : mean_p[0]);
    const unsigned bits = (__float_as_uint(val) + 0x80u) >> 8;
    __hip_atomic_store(xch + cg * 64, tag40 | bits, __ATOMIC_RELAXED, __HIP_MEMORY_SCOPE_AGENT);
  }
  STAMP(6);
  pass2();
  STAMP(7);
  if (half) {
#pragma unroll
    for (int h = 0; h < 2; ++h)
#pragma unroll
      for (int k = 0; k < 2; ++k) {
        u32x4 o = {pk_bf16(az[h][2 * k][0], az[h][2 * k][1]), pk_bf16(az[h][2 * k][2], az[h][2 * k][3]),
                   pk_bf16(az[h][2 * k + 1][0], az[h][2 * k + 1][1]), pk_bf16(az[h][2 * k + 1][2], az[h][2 * k + 1][3])};
        st16(ws + OFF_Z1 + ((size_t)(8 * rp + 2 * w + h) * 28 + 2 * cg + k) * 1024 + lane * 16, o);
      }
    store_ab();
    STAMP(8);
    STAMP_FLUSH(0);
    return;
  }
  u64 gr[14];
#pragma unroll
  for (int c = 0; c < 14; ++c) gr[c] = __hip_atomic_load(xch + c * 64, __ATOMIC_RELAXED, __HIP_MEMORY_SCOPE_AGENT);
  __builtin_amdgcn_sched_barrier(0);
  store_ab();
  __builtin_amdgcn_sched_barrier(0);
  for (unsigned spins = 0;; ++spins) {
    unsigned bh = 0, bl = 0;
#pragma unroll
    for (int c = 0; c < 14; ++c) {
      bh |= (unsigned)(gr[c] >> 32) ^ (unsigned)(tag40 >> 32);
      bl |= (unsigned)gr[c] ^ (unsigned)tag40;
    }
    if (__all((bh | (bl >> 24)) == 0) || spins > (1u << 22)) break;
#pragma unroll
    for (int c = 0; c < 14; ++c) gr[c] = __hip_atomic_load(xch + c * 64, __ATOMIC_RELAXED, __HIP_MEMORY_SCOPE_AGENT);
  }
  STAMP(8);
  if (w < PW2) prewarm(PW1 + w);
  if (cg == 0 && lane == 0) __hip_atomic_store((GAS u64*)(ws + OFF_E + grp * 128), tag, __ATOMIC_RELAXED, __HIP_MEMORY_SCOPE_AGENT);
  float pv[14];
#pragma unroll
  for (int c = 0; c < 14; ++c) pv[c] = __uint_as_float((unsigned)gr[c] << 8);
  float sum = 0.f;
#pragma unroll
  for (int c = 0; c < 14; ++c) sum += pv[c];
  const float mean = sum * (1.0f / 14.0f);
  float dev = 0.f;
#pragma unroll
  for (int c = 0; c < 14; ++c) { float d = pv[c] - mean; dev = __builtin_fmaf(d, d, dev); }
  const float mine = (fq & 2) ? sum : dev * 64.0f;
  const float m2 = mine + __shfl_xor(mine, 32);
  const float rstd = __builtin_amdgcn_rsqf(m2 * (1.0f / 896.0f) + 1e-5f);
  float mu[2], rs[2];
#pragma unroll
  for (int h = 0; h < 2; ++h) {
    rs[h] = __shfl(rstd, fi + 16 * h);
    mu[h] = -__shfl(mean, fi + 16 * h) * rs[h];
  }
  LAS char* sc = lds + w * 4096;
#pragma unroll
  for (int h = 0; h < 2; ++h)
#pragma unroll
    for (int n = 0; n < 4; ++n) {
      const f32x4 v = az[h][n] * rs[h] + mu[h];
      u32x2 o = {pk_bf16(v[0], v[1]), pk_bf16(v[2], v[3])};
      *(LAS u32x2*)(sc + (arow + 4 * h) * 128 + (((4 * n + fq) ^ fi) << 3)) = o;
    }
  WAITL0();
#pragma unroll
  for (int n = 0; n < 4; ++n) {
    const unsigned a = (8 * fq + tq) * 128 + (((4 * n + tp) ^ ((fq << 2) | tq)) << 3);
    bf16x4 lo = __builtin_amdgcn_ds_read_tr16_b64_v4i16((LAS bf16x4*)(sc + a));
    bf16x4 hi = __builtin_amdgcn_ds_read_tr16_b64_v4i16((LAS bf16x4*)(sc + a + 512));
    bf16x8 o = __builtin_shufflevector(lo, hi, 0, 1, 2, 3, 4, 5, 6, 7);
    const int T = 56 + 4 * cg + n, p = T / 7, n7 = T % 7;
    st16(ws + OFF_BT + ((size_t)(p * 64 + 4 * rp + w) * 7 + n7) * 1024 + lane * 16, o);
  }
  STAMP(9);
  STAMP_FLUSH(0);
}

constexpr int STA = 16384, STB = 14336, STG = STA + STB, RING = 5, NT = 32;

constexpr int LA = 4, LEAD = 4;
template <int P>
__device__ __forceinline__ void k2_loader(const char* base, unsigned step, const unsigned (&off)[P], unsigned l0, int cls,
                                          const char* warm, unsigned warm_off, unsigned junk) {
  int nxt = 0, slot = 0;
  auto issue = [&]() {
    const int st = ((nxt & 3) == cls) ? ((nxt + LEAD) & (NT - 1)) : nxt;
    const char* g = base + (size_t)st * step;
    const unsigned l = l0 + slot * STG;
#pragma unroll
    for (int i = 0; i < P; ++i) glds_asm(g, off[i], l + i * 1024);
    ++nxt;
    slot = (slot == RING - 1) ? 0 : slot + 1;
  };
  issue();
  issue();
  WAITV(P);
  BAR();
  issue();
  issue();
#pragma unroll 1
  for (int t = 0; t < NT - LA; ++t) {
    WAITV(P * (LA - 2));
    BAR();
    if (nxt == 24 + cls) touch(warm, warm_off, junk);
    issue();
  }
  WAITV(2 * P); BAR();
  WAITV(P); BAR();
  WAITV(0); BAR();
}

template <int ODD>
__device__ __forceinline__ void k2_epi(f32x4 (&acc)[2][7], bool gate, const float (&bb)[2], const u32x4 (&zq)[2][3],
                                       const u32x2 (&zs)[2], char* op) {
#pragma unroll
  for (int m = 0; m < 2; ++m) {
    if (gate) {
#pragma unroll
      for (int n = 0; n < 7; ++n) {
        const bool single = ODD ? (n == 0) : (n == 6);
        const int nn = n - ODD;
        const unsigned z0 = single ? zs[m][0] : zq[m][(nn < 0 ? 0 : nn) >> 1 < 3 ? (nn < 0 ? 0 : nn) >> 1 : 0][(nn & 1) * 2];
        const unsigned z1 = single ? zs[m][1] : zq[m][(nn < 0 ? 0 : nn) >> 1 < 3 ? (nn < 0 ? 0 : nn) >> 1 : 0][(nn & 1) * 2 + 1];
        acc[m][n][0] = (acc[m][n][0] + bb[m]) * bflo(z0);
        acc[m][n][1] = (acc[m][n][1] + bb[m]) * bfhi(z0);
        acc[m][n][2] = (acc[m][n][2] + bb[m]) * bflo(z1);
        acc[m][n][3] = (acc[m][n][3] + bb[m]) * bfhi(z1);
      }
    }
    char* om = op + (size_t)m * 56 * 1024;
#pragma unroll
    for (int k = 0; k < 3; ++k) {
      const f32x4 a = acc[m][2 * k + ODD], c = acc[m][2 * k + 1 + ODD];
      u32x4 o = {pk_bf16(a[0], a[1]), pk_bf16(a[2], a[3]), pk_bf16(c[0], c[1]), pk_bf16(c[2], c[3])};
      st16(om + (k + ODD) * 1024, o);
    }
    const f32x4 a = acc[m][ODD ? 0 : 6];
    u32x2 o = {pk_bf16(a[0], a[1]), pk_bf16(a[2], a[3])};
    st8(om + (ODD ? 8 : 3 * 1024), o);
  }
}

__global__ __launch_bounds__(512, 2) void k2(const u16* __restrict__ wsr, const u16* __restrict__ wsi, const u16* __restrict__ wsp,
                                             const u16* __restrict__ bsp, char* __restrict__ ws) {
  __shared__ __attribute__((aligned(1024))) char smem[RING * STG + 256];
  LAS char* lds = (LAS char*)smem;
  const int wid = __builtin_amdgcn_readfirstlane(threadIdx.x >> 6), lane = threadIdx.x & 63;
  STAMP_DECL; STAMP(0);
  const int b = blockIdx.x, xcd = b & 7, r = b >> 3;
  int mi, p, cls;
  if (xcd < 4) { mi = xcd * 4 + (r & 3); p = 8 + (r >> 2); cls = ((r >> 2) + r) & 3; }
  else { const int h = xcd - 4; mi = (h & 1) * 8 + (r & 7); p = (h >> 1) * 4 + (r >> 3); cls = ((r >> 3) + r) & 3; }
  if (wid >= 4) {
    const int lw = wid - 4;
    const unsigned ldsb = __builtin_amdgcn_readfirstlane((unsigned)(size_t)lds);
    const unsigned woff = (unsigned)((r * 4 + lw) * 49 + (lane < 49 ? lane : 48)) * 128;
    if (lw < 2) {
      const char* base = (const char*)(p < 4 ? wsr : (p < 8 ? wsi : wsp)) + (size_t)(mi * 128 + lw * 64) * 4096;
      unsigned off[8];
#pragma unroll
      for (int i = 0; i < 8; ++i) off[i] = (unsigned)((lane >> 3) * 4096 + i * 32768 + (((lane & 7) ^ (((lane >> 4) & 1) | ((i & 3) << 1))) << 4));
      k2_loader<8>(base, 128, off, ldsb + lw * 8192, cls, ws + OFF_WO, woff, ldsb + RING * STG);
    } else {
      const char* base = ws + OFF_BT + (size_t)p * 64 * 7 * 1024 + (lw - 2) * 7168;
      unsigned off[7];
#pragma unroll
      for (int i = 0; i < 7; ++i) off[i] = i * 1024 + lane * 16;
      k2_loader<7>(base, STB, off, ldsb + STA + (lw - 2) * 7168, cls, ws + OFF_WO, woff, ldsb + RING * STG);
    }
    STAMP_FLUSH(1);
    return;
  }
  const int fi = lane & 15, fq = lane >> 4;
  const unsigned arow = 8 * (fi >> 2) + (fi & 3);
  const unsigned aoff0 = (32 * wid + arow) * 128 + ((fq ^ (fi >> 1)) << 4);
  const unsigned aoff1 = (32 * wid + arow) * 128 + (((fq + 4) ^ (fi >> 1)) << 4);
  const unsigned boff = STA + lane * 16;
  const int mt0 = mi * 8 + wid * 2;
  const bool gate = p >= 8;
  u32x4 zq[2][3];
  u32x2 zs[2];
  float bb[2] = {bf2f(bsp[mi * 128 + 32 * wid + arow]), bf2f(bsp[mi * 128 + 32 * wid + arow + 4])};
  asm volatile("" : "+v"(bb[0]), "+v"(bb[1]));
  f32x4 acc[2][7];
#pragma unroll
  for (int m = 0; m < 2; ++m)
#pragma unroll
    for (int n = 0; n < 7; ++n) acc[m][n] = (f32x4){0, 0, 0, 0};
  bf16x8 fa[2][2][2], fb[2][2][7];
  auto rd = [&](int set, int slot) {
    LAS char* l = lds + slot * STG;
#pragma unroll
    for (int k = 0; k < 2; ++k) {
#pragma unroll
      for (int m = 0; m < 2; ++m) fa[set][k][m] = *(const LAS bf16x8*)(l + (k ? aoff1 : aoff0) + m * 512);
#pragma unroll
      for (int n = 0; n < 7; ++n) fb[set][k][n] = *(const LAS bf16x8*)(l + boff + k * 7168 + n * 1024);
    }
  };
  auto mm = [&](int set) {
#pragma unroll
    for (int k = 0; k < 2; ++k)
#pragma unroll
      for (int n = 0; n < 7; ++n)
#pragma unroll
        for (int m = 0; m < 2; ++m) acc[m][n] = MFMA(fb[set][k][n], fa[set][k][m], acc[m][n]);
  };
  auto sched = [&]() {
#pragma unroll
    for (int i = 0; i < 18; ++i) {
      __builtin_amdgcn_sched_group_barrier(0x8, 1, 0);
      __builtin_amdgcn_sched_group_barrier(0x100, 1, 0);
    }
    __builtin_amdgcn_sched_group_barrier(0x8, 10, 0);
    __builtin_amdgcn_sched_barrier(0);
  };
#pragma unroll
  for (int m = 0; m < 2; ++m)
#pragma unroll
    for (int n = 0; n < 7; ++n) asm volatile("" : "+v"(acc[m][n]));
  BAR();
  STAMP(1);
  rd(0, 0);
  int slot = 1;
  for (int t = 0; t < NT - 2; t += 2) {
    WAITL0(); BAR();
    rd(1, slot); mm(0); sched();
    slot = (slot == RING - 1) ? 0 : slot + 1;
    WAITL0(); BAR();
    rd(0, slot); mm(1); sched();
    slot = (slot == RING - 1) ? 0 : slot + 1;
  }
  {
    const int odd = p & 1;
    const char* zp = ws + OFF_Z1 + (gate ? ((size_t)mt0 * 28 + ((7 * (p - 8)) >> 1)) * 1024 + lane * 16 : 0);
#pragma unroll
    for (int m = 0; m < 2; ++m) {
      const char* zm = zp + m * 28 * 1024;
#pragma unroll
      for (int k = 0; k < 3; ++k) zq[m][k] = *(const u32x4*)(zm + odd * 1024 + k * 1024);
      zs[m] = *(const u32x2*)(zm + (odd ? 8 : 3 * 1024));
    }
  }
  __builtin_amdgcn_sched_barrier(0);
  WAITL0(); BAR();
  rd(1, slot); mm(0); sched();
  mm(1);
  __builtin_amdgcn_sched_barrier(0);

  STAMP(2);
  char* op = ws + OFF_O2 + ((size_t)mt0 * 56 + ((7 * p) >> 1)) * 1024 + lane * 16;
  if (p & 1) k2_epi<1>(acc, gate, bb, zq, zs, op); else k2_epi<0>(acc, gate, bb, zq, zs, op);
  STAMP(3);
  STAMP_FLUSH(1);
}

__global__ __launch_bounds__(512, 2) void k3(const char* __restrict__ ws, u16* __restrict__ out) {
  __shared__ __attribute__((aligned(1024))) char smem[112 * 1024];
  LAS char* lds = (LAS char*)smem;
  const int wid = __builtin_amdgcn_readfirstlane(threadIdx.x >> 6), lane = threadIdx.x & 63;
  STAMP_DECL; STAMP(0);
  const int b = blockIdx.x, xcd = b & 7, r = b >> 3;
  const int rg = 4 * xcd + (r & 3), dg = r >> 2;
  if (wid >= 4) {
    const int lw = wid - 4;
    const unsigned ldsb = __builtin_amdgcn_readfirstlane((unsigned)(size_t)lds);
    const char* g = ws + OFF_WO + (size_t)dg * 112 * 1024 + lw * 1024;
    auto issue = [&](int c) {
      int cc = c + dg;
      if (cc >= 7) cc -= 7;
#pragma unroll
      for (int k = 0; k < 4; ++k) glds_asm(g + cc * 16384 + k * 4096, lane * 16, ldsb + c * 16384 + k * 4096 + lw * 1024);
    };
    issue(0);
    issue(1);
    STAMP(1);
#pragma unroll
    for (int c = 0; c < 7; ++c) {
      if (c < 6) WAITV(4); else WAITV(0);
      STAMP(2 + c);
      BAR();
      if (c < 5) issue(c + 2);
    }
    STAMP_FLUSH(2);
    return;
  }
  const int mt = 4 * rg + wid, fi = lane & 15, fq = lane >> 4;
  const char* o2 = ws + OFF_O2 + (size_t)mt * 56 * 1024 + lane * 16;
  bf16x8 hf[7][4];
  auto ldh = [&](int c) {
    int cc = c + dg;
    if (cc >= 7) cc -= 7;
#pragma unroll
    for (int k = 0; k < 4; ++k) hf[c][k] = *(const bf16x8*)(o2 + (28 + cc * 4 + k) * 1024);
  };
  ldh(0);
  ldh(1);
  u32x4 yr[2], yi[2];
  const int t = 32 * (mt >> 1) + 8 * (fi >> 2) + 4 * (mt & 1) + (fi & 3);
  float g;
  f32x4 acc[4];
#pragma unroll
  for (int n = 0; n < 4; ++n) acc[n] = (f32x4){0, 0, 0, 0};
  bf16x8 wf[2][16];
#pragma unroll
  for (int c = 0; c < 8; ++c) {
    __builtin_amdgcn_sched_barrier(0);
    if (c < 7) BAR();
    if (c == 0) STAMP(1);
    if (c == 6) STAMP(2);
    if (c < 5) ldh(c + 2);
    if (c == 5) {
#pragma unroll
      for (int k = 0; k < 2; ++k) {
        yr[k] = *(const u32x4*)(o2 + (2 * dg + k) * 1024);
        yi[k] = *(const u32x4*)(o2 + (14 + 2 * dg + k) * 1024);
      }
      g = ((const float*)(ws + OFF_G))[t];
    }
    __builtin_amdgcn_sched_barrier(0);
    if (c < 7) {
#pragma unroll
      for (int i = 0; i < 16; ++i) wf[c & 1][i] = *(const LAS bf16x8*)(lds + c * 16384 + i * 1024 + lane * 16);
    }
    if (c > 0) {
#pragma unroll
      for (int i = 0; i < 16; ++i) acc[i & 3] = MFMA(wf[(c - 1) & 1][i], hf[c - 1][i >> 2], acc[i & 3]);
      if (c < 7) {
#pragma unroll
        for (int i = 0; i < 16; ++i) {
          __builtin_amdgcn_sched_group_barrier(0x8, 1, 0);
          __builtin_amdgcn_sched_group_barrier(0x100, 1, 0);
        }
      }
    }
  }
  STAMP(3);
  const float g1 = 1.0f - g;
#pragma unroll
  for (int k = 0; k < 2; ++k) {
    float v[8];
#pragma unroll
    for (int e = 0; e < 4; ++e) {
      const float a0 = acc[2 * k + (e >> 1)][(e & 1) * 2], a1 = acc[2 * k + (e >> 1)][(e & 1) * 2 + 1];
      v[2 * e] = g * (bflo(yr[k][e]) - bflo(yi[k][e])) + g1 * a0;
      v[2 * e + 1] = g * (bfhi(yr[k][e]) - bfhi(yi[k][e])) + g1 * a1;
    }
    u32x4 o = {pk_bf16(v[0], v[1]), pk_bf16(v[2], v[3]), pk_bf16(v[4], v[5]), pk_bf16(v[6], v[7])};
    *(u32x4*)(out + (size_t)t * D + 64 * dg + 32 * k + 8 * fq) = o;
  }
  STAMP(4);
  STAMP_FLUSH(2);
}

extern "C" void kernel_launch(void* const* d_in, const int* in_sizes, int n_in, void* d_out, int out_size, void* d_ws,
                              size_t ws_size, hipStream_t stream) {
  const u16* x = (const u16*)d_in[0];
  const u16* wsr = (const u16*)d_in[1];
  const u16* wsi = (const u16*)d_in[2];
  const u16* wdr = (const u16*)d_in[3];
  const u16* wdi = (const u16*)d_in[4];
  const u16* win = (const u16*)d_in[6];
  const u16* wsp = (const u16*)d_in[7];
  const u16* bsp = (const u16*)d_in[8];
  const u16* wout = (const u16*)d_in[9];
  const u16* gw = (const u16*)d_in[10];
  char* ws = (char*)d_ws;
  k1<<<256, 512, 0, stream>>>(x, wdr, wdi, win, wout, gw, wsr, wsi, wsp, ws);
  k2<<<256, 512, 0, stream>>>(wsr, wsi, wsp, bsp, ws);
  k3<<<224, 512, 0, stream>>>(ws, (u16*)d_out);
}
```

```cpp
#include <hip/hip_runtime.h>
#include <hip/hip_bf16.h>

typedef unsigned short u16;
typedef unsigned long long u64;
typedef short bf16x8 __attribute__((ext_vector_type(8)));
typedef short bf16x4 __attribute__((ext_vector_type(4)));
typedef float f32x2 __attribute__((ext_vector_type(2)));
typedef float f32x4 __attribute__((ext_vector_type(4)));
typedef unsigned u32x2 __attribute__((ext_vector_type(2)));
typedef unsigned u32x4 __attribute__((ext_vector_type(4)));
#define LAS __attribute__((address_space(3)))
#define GAS __attribute__((address_space(1)))
#define RLX_AGENT __ATOMIC_RELAXED, __HIP_MEMORY_SCOPE_AGENT

#define WAITV(n) asm volatile("s_waitcnt vmcnt(%0)" ::"n"(n) : "memory")
#define WAITL0() asm volatile("s_waitcnt lgkmcnt(0)" ::: "memory")
#define BAR() do { asm volatile("" ::: "memory"); __builtin_amdgcn_s_barrier(); asm volatile("" ::: "memory"); } while (0)
#define SCHED() __builtin_amdgcn_sched_barrier(0)
#define SGB(mask, n) __builtin_amdgcn_sched_group_barrier(mask, n, 0)
#define MFMA(a, b, c) __builtin_amdgcn_mfma_f32_16x16x32_bf16(a, b, c, 0, 0, 0)
#define TRD(p) __builtin_amdgcn_ds_read_tr16_b64_v4i16((LAS bf16x4*)(p))
#define CAT(lo, hi) __builtin_shufflevector(lo, hi, 0, 1, 2, 3, 4, 5, 6, 7)

template <bool SC1 = false>
__device__ __forceinline__ void glds(const void* sbase, unsigned voff, unsigned l) {
  unsigned keep;
  l = __builtin_amdgcn_readfirstlane(l);
  if (SC1)
    asm volatile("s_mov_b32 %0, m0\n\ts_mov_b32 m0, %3\n\ts_nop 0\n\tglobal_load_lds_dwordx4 %1, %2 sc1\n\ts_mov_b32 m0, %0"
                 : "=&s"(keep) : "v"(voff), "s"(sbase), "s"(l) : "memory");
  else
    asm volatile("s_mov_b32 %0, m0\n\ts_mov_b32 m0, %3\n\ts_nop 0\n\tglobal_load_lds_dwordx4 %1, %2\n\ts_mov_b32 m0, %0"
                 : "=&s"(keep) : "v"(voff), "s"(sbase), "s"(l) : "memory");
}
__device__ __forceinline__ void touch(const void* sbase, unsigned voff, unsigned l) {
  unsigned keep;
  l = __builtin_amdgcn_readfirstlane(l);
  asm volatile("s_mov_b32 %0, m0\n\ts_mov_b32 m0, %3\n\ts_nop 0\n\tglobal_load_lds_dword %1, %2\n\ts_mov_b32 m0, %0"
               : "=&s"(keep) : "v"(voff), "s"(sbase), "s"(l) : "memory");
}

constexpr int S = 2048, D = 448;

__device__ __forceinline__ float bf2f(u16 v) { return __uint_as_float(((unsigned)v) << 16); }
__device__ __forceinline__ float bflo(unsigned v) { return __uint_as_float(v << 16); }
__device__ __forceinline__ float bfhi(unsigned v) { return __uint_as_float(v & 0xffff0000u); }
__device__ __forceinline__ unsigned pk_bf16(float lo, float hi) {
  typedef __bf16 bf2 __attribute__((ext_vector_type(2)));
  f32x2 v = {lo, hi};
  bf2 b = __builtin_convertvector(v, bf2);
  return *(unsigned*)&b;
}
__device__ __forceinline__ f32x2 gelu2(f32x2 v) {
  const float c0 = -2.0f * 0.7978845608028654f * 1.4426950408889634f;
  const float c1 = c0 * 0.044715f;
  const f32x2 u = v * ((v * v) * c1 + c0);
  f32x2 e = {__builtin_amdgcn_exp2f(u.x), __builtin_amdgcn_exp2f(u.y)};
  e = e + 1.0f;
  const f32x2 rc = {__builtin_amdgcn_rcpf(e.x), __builtin_amdgcn_rcpf(e.y)};
  return v * rc;
}
__device__ __forceinline__ f32x4 gelu4(f32x4 v) {
  const f32x2 lo = gelu2((f32x2){v[0], v[1]}), hi = gelu2((f32x2){v[2], v[3]});
  return (f32x4){lo.x, lo.y, hi.x, hi.y};
}
__device__ __forceinline__ float xsum(float s) {
  auto a = __builtin_amdgcn_permlane16_swap(__float_as_uint(s), __float_as_uint(s), false, false);
  s = __uint_as_float(a[0]) + __uint_as_float(a[1]);
  auto b = __builtin_amdgcn_permlane32_swap(__float_as_uint(s), __float_as_uint(s), false, false);
  return __uint_as_float(b[0]) + __uint_as_float(b[1]);
}

constexpr unsigned OFF_BT = 0;
constexpr unsigned OFF_O2 = OFF_BT + 1792 * S * 2;
constexpr unsigned OFF_G = OFF_O2 + 1792 * S * 2;
constexpr unsigned OFF_X = OFF_G + 8192;
constexpr unsigned OFF_F1 = OFF_X + 64 * 8 * 512;
constexpr unsigned OFF_F2 = OFF_F1 + 8192;
constexpr unsigned OFF_E = OFF_F2 + 8192;
constexpr unsigned WS_USED = OFF_E + 128;
constexpr u64 EPOCH_STEP = 0x9E3779B97F4A7C15ull;
constexpr unsigned SPIN_CAP = 1u << 20;

#define STAMP_DECL
#define STAMP(i)
#define STAMP_FLUSH()

constexpr unsigned WR0 = 114688, WSL = 14336;
constexpr int STA = 16384, STB = 14336, STG = STA + STB, RING = 4, NT = 32, LEAD = 4;
constexpr unsigned SPARE = RING * STG, JUNK = SPARE + 32768;
__device__ __forceinline__ constexpr unsigned wo_loc(int c) { return c < 2 ? SPARE + c * 16384 : (c - 2) * 16384; }

__device__ __forceinline__ void wait_flags(GAS u64* p, u64 tag) {
  for (unsigned spins = 0; spins < SPIN_CAP; ++spins) {
    const u64 v = __hip_atomic_load(p, RLX_AGENT);
    if (__all(v == tag)) break;
    __builtin_amdgcn_s_sleep(1);
  }
  __builtin_amdgcn_fence(__ATOMIC_ACQUIRE, "wavefront");
}

struct Loader2 {
  const char* base; unsigned step, l0; int cls, nxt, slot;
  template <int P, bool SC1> __device__ __forceinline__ void issue(const unsigned (&off)[P]) {
    const int st = ((nxt & 3) == cls) ? ((nxt + LEAD) & (NT - 1)) : nxt;
    const char* g = base + (size_t)st * step;
    const unsigned l = l0 + slot * STG;
#pragma unroll
    for (int i = 0; i < P; ++i) glds<SC1>(g, off[i], l + i * 1024);
    ++nxt;
    slot = (slot + 1) & (RING - 1);
  }
};

__global__ __launch_bounds__(512, 2) void fused(const u16* __restrict__ x, const u16* __restrict__ wsr, const u16* __restrict__ wsi,
                                                const u16* __restrict__ wdr, const u16* __restrict__ wdi, const u16* __restrict__ win,
                                                const u16* __restrict__ wsp, const u16* __restrict__ bsp, const u16* __restrict__ wout,
                                                const u16* __restrict__ gw, char* __restrict__ ws, u16* __restrict__ out) {
  __shared__ __attribute__((aligned(1024))) char smem[163840];
  LAS char* lds = (LAS char*)smem;
  const int wid = __builtin_amdgcn_readfirstlane(threadIdx.x >> 6), lane = threadIdx.x & 63;
  STAMP_DECL; STAMP(0);
  const int b = blockIdx.x, xcd = b & 7, r = b >> 3;
  int mi, p, cls;
  if (xcd < 4) { mi = xcd * 4 + (r & 3); p = 8 + (r >> 2); cls = ((r >> 2) + r) & 3; }
  else { const int h = xcd - 4; mi = (h & 1) * 8 + (r & 7); p = (h >> 1) * 4 + (r >> 3); cls = ((r >> 3) + r) & 3; }
  const bool G = xcd < 4;
  const int N1 = G ? 14 : 7;
  const u64 ord = (u64)(cls == 0 ? 0x5463120u : cls == 1 ? 0x6532410u : cls == 2 ? 0x6542031u : 0x6423501u) * 0x10000001ull;
  const int rg = 4 * xcd + (r & 3), dg = r >> 2;
  u64 tag = *(const __attribute__((address_space(4))) u64*)(ws + OFF_E);
  const unsigned ldsb = __builtin_amdgcn_readfirstlane((unsigned)(size_t)lds);
  const unsigned l3 = lane >> 3, l7 = lane & 7, l4 = (lane >> 4) & 1;

  if (wid >= 4) {
    const int lw = wid - 4;
    const unsigned wo_off = l3 * 896 + (((((l7 >> 1) ^ ((lane >> 4) & 3)) << 1) | (l7 & 1)) << 4);
    const char* wo_base = (const char*)wout + (dg < 7 ? dg : 0) * 128 + (size_t)lw * 32 * 896;
    auto wo_chunk = [&](int c) {
      int cc = c + dg;
      if (cc >= 7) cc -= 7;
#pragma unroll
      for (int i = 0; i < 4; ++i) glds(wo_base + (size_t)(cc * 128 + i * 8) * 896, wo_off, ldsb + wo_loc(c) + lw * 4096 + i * 1024);
    };
    const unsigned warm_off = (unsigned)((r * 4 + lw) * 49 + (lane < 49 ? lane : 48)) * 128;
    if (lw < 2) {
      unsigned off[8];
      {
        const char* xg = (const char*)x + (size_t)(mi * 128 + lw * 64) * 896;
#pragma unroll
        for (int i = 0; i < 8; ++i) off[i] = (8 * i + l3) * 896 + ((l7 ^ (l4 | ((i & 3) << 1))) << 4);
        auto issue = [&](int s) {
          const unsigned st = (ord >> (4 * s)) & 7;
#pragma unroll
          for (int i = 0; i < 8; ++i) glds(xg + st * 128, off[i], ldsb + st * 16384 + lw * 8192 + i * 1024);
        };
        issue(0);
        issue(1);
        asm volatile("" : "+s"(tag));
        STAMP(1);
        WAITV(8);
        BAR();
        issue(2);
#pragma unroll 1
        for (int s = 0; s < 4; ++s) { WAITV(8); BAR(); issue(s + 3); }
        WAITV(8); BAR();
        WAITV(0);
#pragma unroll 1
        for (int s = 5; s < N1; ++s) BAR();
      }
      STAMP(2);
      const u16* am = p < 4 ? wsr : wsi;
      am = G ? wsp : am;
#pragma unroll
      for (int i = 0; i < 8; ++i) off[i] = (8 * i + l3) * 4096 + ((l7 ^ (l4 | ((i & 3) << 1))) << 4);
      Loader2 L{(const char*)am + (size_t)(mi * 128 + lw * 64) * 4096, 128, ldsb + lw * 8192, cls, 0, 0};
      L.issue<8, false>(off);
      L.issue<8, false>(off);
      if (!G) {
        const int row0 = mi * 128 + p * 16 + lw * 8;
        const int lc = lane < 56 ? lane : 55;
        const u32x4 gv = *(const u32x4*)(gw + lc * 8);
        u32x4 xv[8];
#pragma unroll
        for (int i = 0; i < 8; ++i) xv[i] = *(const u32x4*)(x + (size_t)(row0 + i) * D + lc * 8);
        float res = 0.f;
#pragma unroll
        for (int i = 0; i < 8; ++i) {
          float a = 0.f;
#pragma unroll
          for (int j = 0; j < 4; ++j) a += bflo(xv[i][j]) * bflo(gv[j]) + bfhi(xv[i][j]) * bfhi(gv[j]);
          a = lane < 56 ? a : 0.f;
#pragma unroll
          for (int o = 8; o >= 1; o >>= 1) a += __shfl_xor(a, o);
          a = xsum(a);
          if (lane == i) res = a;
        }
        if (lane < 8) __hip_atomic_store((GAS float*)(ws + OFF_G) + row0 + lane, 1.0f / (1.0f + __expf(-res)), RLX_AGENT);
      }
      L.issue<8, false>(off);
      L.issue<8, false>(off);
      WAITV(24);
      STAMP(3);
      BAR();
#pragma unroll 1
      for (int t = 0; t < NT - 4; ++t) {
        WAITV(16);
        BAR();
        if (L.nxt == 20 + cls) touch(wout, warm_off, ldsb + JUNK);
        L.issue<8, false>(off);
      }
      wo_chunk(0); wo_chunk(1);
      WAITV(24); BAR();
      wo_chunk(2);
      WAITV(20); BAR();
      wo_chunk(3); wo_chunk(4);
      WAITV(20); BAR();
    } else {
      const int l2 = lw - 2;
      unsigned off[7];
      {
        const char* wb; unsigned step;
        if (G) {
          wb = (const char*)win + (p - 8) * 224 + 1792; step = 64 * 3584;
#pragma unroll
          for (int i = 0; i < 7; ++i) {
            const unsigned u = (l2 * 7 + i) * 64 + lane, R = u / 14, c = u % 14;
            off[i] = (R ^ (((R >> 3) & 1) << 2)) * 3584 + c * 16;
          }
        } else {
          const u16* dm = p < 4 ? wdr : wdi;
          wb = (const char*)dm + (size_t)(p & 3) * 112 * 896; step = 128;
#pragma unroll
          for (int i = 0; i < 7; ++i) {
            const unsigned lr = 8 * (l2 * 7 + i) + l3;
            off[i] = lr * 896 + ((l7 ^ ((lr >> 1) & 7)) << 4);
          }
        }
        int slot = 0;
        auto issue = [&](int s) {
          const unsigned st = (ord >> (4 * s)) & 7;
          const char* g = wb + st * step - (s >= 7 ? 1792 : 0);
#pragma unroll
          for (int i = 0; i < 7; ++i) glds(g, off[i], ldsb + WR0 + slot * WSL + l2 * 7168 + i * 1024);
          slot = slot == 2 ? 0 : slot + 1;
        };
        issue(0);
        issue(1);
        asm volatile("" : "+s"(tag));
        STAMP(1);
        WAITV(7);
        BAR();
        issue(2);
#pragma unroll 1
        for (int s = 0; s < N1 - 3; ++s) { WAITV(7); BAR(); issue(s + 3); }
        WAITV(7); BAR();
        WAITV(0); BAR();
        BAR();
      }
      STAMP(2);
      tag += EPOCH_STEP;
#pragma unroll
      for (int i = 0; i < 7; ++i) off[i] = i * 1024 + lane * 16;
      Loader2 L{ws + OFF_BT + (size_t)p * 64 * 7 * 1024 + l2 * 7168, STB, ldsb + STA + l2 * 7168, cls, 0, 0};
      wait_flags((GAS u64*)(ws + OFF_F1) + p * 64 + lane, tag);
      STAMP(4);
      L.issue<7, true>(off);
      L.issue<7, true>(off);
      WAITV(7);
      STAMP(3);
      BAR();
      L.issue<7, true>(off);
      L.issue<7, true>(off);
#pragma unroll 1
      for (int t = 0; t < NT - 4; ++t) {
        WAITV(14);
        BAR();
        if (L.nxt == 20 + cls) touch(wout, warm_off, ldsb + JUNK);
        L.issue<7, true>(off);
      }
      wo_chunk(0); wo_chunk(1);
      WAITV(22); BAR();
      wo_chunk(2);
      WAITV(19); BAR();
      wo_chunk(3); wo_chunk(4);
      WAITV(20); BAR();
    }
    wo_chunk(5); wo_chunk(6);
    WAITV(0);
    STAMP(5);
    BAR();
    STAMP_FLUSH();
    return;
  }

  const int fi = lane & 15, fq = lane >> 4;
  const unsigned arow = 8 * (fi >> 2) + (fi & 3);
  const unsigned tq = fi >> 2, tp = lane & 3;
  const unsigned aoff0 = (32 * wid + arow) * 128 + ((fq ^ (fi >> 1)) << 4);
  const unsigned aoff1 = (32 * wid + arow) * 128 + (((fq + 4) ^ (fi >> 1)) << 4);
  const __amdgpu_buffer_rsrc_t rws = __builtin_amdgcn_make_buffer_rsrc(ws, 0, WS_USED, 0x00020000);
  float bb[2] = {bf2f(bsp[mi * 128 + 32 * wid + arow]), bf2f(bsp[mi * 128 + 32 * wid + arow + 4])};
  asm volatile("" : "+v"(bb[0]), "+v"(bb[1]), "+s"(tag));
  tag += EPOCH_STEP;
  f32x4 acc[2][7];
  auto zero_acc = [&]() {
#pragma unroll
    for (int m = 0; m < 2; ++m)
#pragma unroll
      for (int n = 0; n < 7; ++n) { acc[m][n] = (f32x4){0, 0, 0, 0}; asm volatile("" : "+v"(acc[m][n])); }
  };
  zero_acc();
  u32x2 z1p[2][7];
  const unsigned bt_off = OFF_BT + ((p * 64 + 4 * mi + wid) * 7) * 1024;
  GAS u64* f1 = (GAS u64*)(ws + OFF_F1) + p * 64 + 4 * mi + wid;
  if (G) {
    bf16x8 fa[2][2];
    bf16x4 fl[2][7], fh[2][7];
    const unsigned b0 = WR0 + (8 * fq + 4 * (fq & 1) + tq) * 224 + tp * 8;
    const unsigned b1 = WR0 + (8 * fq + 4 * (1 - (fq & 1)) + tq) * 224 + tp * 8;
    int slot = 0;
    auto rd = [&](int k, int s, int sl) {
      LAS char* lx = lds + ((ord >> (4 * s)) & 7) * 16384 + (k ? aoff1 : aoff0);
      LAS char* lw = lds + sl * WSL + k * 7168;
#pragma unroll
      for (int h = 0; h < 2; ++h) fa[k][h] = *(const LAS bf16x8*)(lx + h * 512);
#pragma unroll
      for (int n = 0; n < 7; ++n) { fl[k][n] = TRD(lw + b0 + n * 32); fh[k][n] = TRD(lw + b1 + n * 32); }
    };
    auto mm = [&](int k) {
#pragma unroll
      for (int n = 0; n < 7; ++n)
#pragma unroll
        for (int h = 0; h < 2; ++h) acc[h][n] = MFMA(CAT(fl[k][n], fh[k][n]), fa[k][h], acc[h][n]);
      SGB(0x8, 1); SGB(0x100, 2); SGB(0x8, 1); SGB(0x100, 2);
#pragma unroll
      for (int i = 0; i < 12; ++i) { SGB(0x8, 1); SGB(0x100, 1); }
      SCHED();
    };
    auto run = [&](int s0, int s1) {
#pragma unroll 1
      for (int s = s0; s < s1; ++s) {
        rd(1, s, slot); mm(0);
        slot = slot == 2 ? 0 : slot + 1;
        WAITL0(); BAR();
        rd(0, s + 1, slot); mm(1);
      }
    };
    BAR();
    STAMP(1);
    rd(0, 0, 0);
    SCHED();
    run(0, 7);
    STAMP(2);
    f32x4 z2[2][7];
    GAS u64* xch = (GAS u64*)(ws + OFF_X) + (mi * 4 + wid) * 8 * 64 + lane;
    const u64 tag40 = tag << 24;
    {
      float mean_p[2], m2_p[2];
#pragma unroll
      for (int h = 0; h < 2; ++h) {
        f32x4 s4 = {0, 0, 0, 0};
#pragma unroll
        for (int n = 0; n < 7; ++n) { z2[h][n] = gelu4(acc[h][n]); s4 += z2[h][n]; }
        mean_p[h] = xsum((s4[0] + s4[1]) + (s4[2] + s4[3])) * (1.0f / 112.0f);
      }
#pragma unroll
      for (int h = 0; h < 2; ++h) {
        f32x4 q4 = {0, 0, 0, 0};
#pragma unroll
        for (int n = 0; n < 7; ++n) { const f32x4 d = z2[h][n] - mean_p[h]; q4 += d * d; }
        m2_p[h] = xsum((q4[0] + q4[1]) + (q4[2] + q4[3]));
      }
      const float val = (fq & 2) ? ((fq & 1) ? m2_p[1] : m2_p[0]) : ((fq & 1) ? mean_p[1] : mean_p[0]);
      const unsigned bits = (__float_as_uint(val) + 0x80u) >> 8;
      __hip_atomic_store(xch + (p - 8) * 64, tag40 | bits, RLX_AGENT);
    }
    zero_acc();
    STAMP(3);
    run(7, 10);
    u64 gr[8];
#pragma unroll
    for (int c = 0; c < 8; ++c) gr[c] = __hip_atomic_load(xch + c * 64, RLX_AGENT);
    SCHED();
    run(10, 11);
    STAMP(4);
    for (unsigned spins = 0;; ++spins) {
      unsigned bh = 0, bl = 0;
#pragma unroll
      for (int c = 0; c < 8; ++c) {
        bh |= (unsigned)(gr[c] >> 32) ^ (unsigned)(tag40 >> 32);
        bl |= (unsigned)gr[c] ^ (unsigned)tag40;
      }
      if (__all((bh | (bl >> 24)) == 0) || spins > SPIN_CAP) break;
#pragma unroll
      for (int c = 0; c < 8; ++c) gr[c] = __hip_atomic_load(xch + c * 64, RLX_AGENT);
    }
    STAMP(5);
    {
      float pv[8], sum = 0.f, dev = 0.f;
#pragma unroll
      for (int c = 0; c < 8; ++c) { pv[c] = __uint_as_float((unsigned)gr[c] << 8); sum += pv[c]; }
      const float mean = sum * 0.125f;
#pragma unroll
      for (int c = 0; c < 8; ++c) { const float d = pv[c] - mean; dev = __builtin_fmaf(d, d, dev); }
      const float mine = (fq & 2) ? sum : dev * 112.0f;
      auto sm = __builtin_amdgcn_permlane32_swap(__float_as_uint(mine), __float_as_uint(mine), false, false);
      const float rstd = __builtin_amdgcn_rsqf((__uint_as_float(sm[0]) + __uint_as_float(sm[1])) * (1.0f / 896.0f) + 1e-5f);
      auto ml = __builtin_amdgcn_permlane32_swap(__float_as_uint(mean), __float_as_uint(mean), false, false);
      const float nm = -__uint_as_float(ml[0]) * rstd;
      auto rs = __builtin_amdgcn_permlane16_swap(__float_as_uint(rstd), __float_as_uint(rstd), false, false);
      auto mu = __builtin_amdgcn_permlane16_swap(__float_as_uint(nm), __float_as_uint(nm), false, false);
      LAS char* sc = lds + ((ord >> (4 * (wid >> 1))) & 7) * 16384 + (wid & 1) * 8192;
      const unsigned wsw = ((fi & 7) << 2) ^ (fi >> 2);
#pragma unroll
      for (int h = 0; h < 2; ++h)
#pragma unroll
        for (int n = 0; n < 7; ++n) {
          const f32x4 v = z2[h][n] * __uint_as_float(rs[h]) + __uint_as_float(mu[h]);
          const u32x2 o = {pk_bf16(v[0], v[1]), pk_bf16(v[2], v[3])};
          *(LAS u32x2*)(sc + (arow + 4 * h) * 256 + (((4 * n + fq) ^ wsw) << 3)) = o;
        }
      WAITL0();
      const unsigned ri = (fq << 2) | tq, rsw = ((ri & 7) << 2) ^ (ri >> 2);
#pragma unroll
      for (int n = 0; n < 7; ++n) {
        const unsigned a = (8 * fq + tq) * 256 + (((4 * n + tp) ^ rsw) << 3);
        const bf16x8 o = CAT(TRD(sc + a), TRD(sc + a + 1024));
        __builtin_amdgcn_raw_buffer_store_b128(*(const u32x4*)&o, rws, lane * 16, bt_off + n * 1024, 16);
      }
    }
    SCHED();
    STAMP(6);
    run(11, 13);
    WAITV(0);
    if (lane == 0) __hip_atomic_store(f1, tag, RLX_AGENT);
    STAMP(7);
    run(13, 14);
    STAMP(8);
#pragma unroll
    for (int h = 0; h < 2; ++h)
#pragma unroll
      for (int n = 0; n < 7; ++n) {
        const f32x4 v = gelu4(acc[h][n]);
        z1p[h][n] = (u32x2){pk_bf16(v[0], v[1]), pk_bf16(v[2], v[3])};
      }
  } else {
    bf16x8 fa[2][2], fb[2][7];
    const unsigned b0 = WR0 + fi * 128 + ((fq ^ (fi >> 1)) << 4);
    const unsigned b1 = WR0 + fi * 128 + (((fq + 4) ^ (fi >> 1)) << 4);
    int slot = 0;
    auto rd = [&](int k, int s, int sl) {
      LAS char* lx = lds + ((ord >> (4 * s)) & 7) * 16384 + (k ? aoff1 : aoff0);
      LAS char* lw = lds + sl * WSL + (k ? b1 : b0);
#pragma unroll
      for (int h = 0; h < 2; ++h) fa[k][h] = *(const LAS bf16x8*)(lx + h * 512);
#pragma unroll
      for (int n = 0; n < 7; ++n) fb[k][n] = *(const LAS bf16x8*)(lw + n * 2048);
    };
    auto mm = [&](int k) {
#pragma unroll
      for (int n = 0; n < 7; ++n)
#pragma unroll
        for (int h = 0; h < 2; ++h) acc[h][n] = MFMA(fa[k][h], fb[k][n], acc[h][n]);
#pragma unroll
      for (int i = 0; i < 9; ++i) { SGB(0x8, 1); SGB(0x100, 1); }
      SGB(0x8, 5);
      SCHED();
    };
    BAR();
    STAMP(1);
    rd(0, 0, 0);
    SCHED();
#pragma unroll 1
    for (int s = 0; s < 7; ++s) {
      rd(1, s, slot); mm(0);
      slot = slot == 2 ? 0 : slot + 1;
      WAITL0(); BAR();
      rd(0, s + 1, slot); mm(1);
    }
    STAMP(2);
#pragma unroll
    for (int n = 0; n < 7; ++n) {
      const u32x4 o = {pk_bf16(acc[0][n][0], acc[0][n][1]), pk_bf16(acc[0][n][2], acc[0][n][3]),
                       pk_bf16(acc[1][n][0], acc[1][n][1]), pk_bf16(acc[1][n][2], acc[1][n][3])};
      __builtin_amdgcn_raw_buffer_store_b128(o, rws, lane * 16, bt_off + n * 1024, 16);
    }
    STAMP(6);
    WAITV(0);
    if (lane == 0) __hip_atomic_store(f1, tag, RLX_AGENT);
    STAMP(7);
#pragma unroll
    for (int h = 0; h < 2; ++h)
#pragma unroll
      for (int n = 0; n < 7; ++n) z1p[h][n] = (u32x2){0x3f803f80u, 0x3f803f80u};
    bb[0] = bb[1] = 0.f;
  }
  zero_acc();
  {
    const unsigned boff = STA + lane * 16;
    bf16x8 fa[2][2][2], fb[2][2][7];
    auto rd = [&](int set, int slot) {
      LAS char* l = lds + slot * STG;
#pragma unroll
      for (int k = 0; k < 2; ++k) {
#pragma unroll
        for (int m = 0; m < 2; ++m) fa[set][k][m] = *(const LAS bf16x8*)(l + (k ? aoff1 : aoff0) + m * 512);
#pragma unroll
        for (int n = 0; n < 7; ++n) fb[set][k][n] = *(const LAS bf16x8*)(l + boff + k * 7168 + n * 1024);
      }
    };
    auto mm = [&](int set) {
#pragma unroll
      for (int k = 0; k < 2; ++k)
#pragma unroll
        for (int n = 0; n < 7; ++n)
#pragma unroll
          for (int m = 0; m < 2; ++m) acc[m][n] = MFMA(fb[set][k][n], fa[set][k][m], acc[m][n]);
    };
    auto sched = [&]() {
#pragma unroll
      for (int i = 0; i < 18; ++i) { SGB(0x8, 1); SGB(0x100, 1); }
      SGB(0x8, 10);
      SCHED();
    };
    STAMP(9);
    WAITL0();
    BAR();
    STAMP(10);
    rd(0, 0);
    int slot = 1;
#pragma unroll 1
    for (int t = 0; t < NT - 2; t += 2) {
      WAITL0(); BAR();
      rd(1, slot); mm(0); sched();
      slot = (slot + 1) & (RING - 1);
      WAITL0(); BAR();
      rd(0, slot); mm(1); sched();
      slot = (slot + 1) & (RING - 1);
    }
    WAITL0(); BAR();
    rd(1, slot); mm(0); sched();
    mm(1);
    SCHED();
  }
  STAMP(11);
  {
    const int odd = p & 1;
    u32x2 o[2][7];
#pragma unroll
    for (int m = 0; m < 2; ++m)
#pragma unroll
      for (int n = 0; n < 7; ++n) {
        const f32x4 z = {bflo(z1p[m][n][0]), bfhi(z1p[m][n][0]), bflo(z1p[m][n][1]), bfhi(z1p[m][n][1])};
        const f32x4 v = (acc[m][n] + bb[m]) * z;
        o[m][n] = (u32x2){pk_bf16(v[0], v[1]), pk_bf16(v[2], v[3])};
      }
    const unsigned o2 = OFF_O2 + ((mi * 8 + wid * 2) * 56 + ((7 * p) >> 1)) * 1024;
#pragma unroll
    for (int m = 0; m < 2; ++m) {
      const unsigned om = o2 + m * 56 * 1024;
      if (odd) {
        __builtin_amdgcn_raw_buffer_store_b64(o[m][0], rws, lane * 16 + 8, om, 16);
#pragma unroll
        for (int k = 0; k < 3; ++k)
          __builtin_amdgcn_raw_buffer_store_b128((u32x4){o[m][2 * k + 1][0], o[m][2 * k + 1][1], o[m][2 * k + 2][0], o[m][2 * k + 2][1]},
                                                 rws, lane * 16, om + (k + 1) * 1024, 16);
      } else {
#pragma unroll
        for (int k = 0; k < 3; ++k)
          __builtin_amdgcn_raw_buffer_store_b128((u32x4){o[m][2 * k][0], o[m][2 * k][1], o[m][2 * k + 1][0], o[m][2 * k + 1][1]},
                                                 rws, lane * 16, om + k * 1024, 16);
        __builtin_amdgcn_raw_buffer_store_b64(o[m][6], rws, lane * 16, om + 3 * 1024, 16);
      }
    }
  }
  STAMP(12);
  WAITV(0);
  if (lane == 0) __hip_atomic_store((GAS u64*)(ws + OFF_F2) + (4 * mi + wid) * 16 + p, tag, RLX_AGENT);
  STAMP(13);
  BAR();
  if (r >= 28) { STAMP_FLUSH(); return; }
  const int mt = 4 * rg + wid;
  unsigned wb[4];
#pragma unroll
  for (int n = 0; n < 4; ++n) wb[n] = (4 * fq + tq) * 128 + ((n ^ (((4 * fq + tq) >> 1) & 3)) << 5) + tp * 8;
  bf16x4 wl[2][16], wh[2][16];
  auto rdw = [&](int c) {
#pragma unroll
    for (int i = 0; i < 16; ++i) {
      LAS char* a = lds + wb[i & 3] + (wo_loc(c) + (i >> 2) * 4096);
      wl[c & 1][i] = TRD(a);
      wh[c & 1][i] = TRD(a + 2048);
    }
  };
  rdw(0);
  SCHED();
  wait_flags((GAS u64*)(ws + OFF_F2) + (mt >> 1) * 16 + fi, tag);
  STAMP(14);
  if (b == 0 && threadIdx.x == 0) __hip_atomic_store((GAS u64*)(ws + OFF_E), tag, RLX_AGENT);
  const unsigned o2 = OFF_O2 + mt * 56 * 1024;
  u32x4 hf[7][4];
  auto ldh = [&](int c) {
    int cc = c + dg;
    if (cc >= 7) cc -= 7;
#pragma unroll
    for (int k = 0; k < 4; ++k) hf[c][k] = __builtin_amdgcn_raw_buffer_load_b128(rws, lane * 16 + k * 1024, o2 + (28 + cc * 4) * 1024, 16);
  };
  ldh(0); ldh(1); ldh(2); ldh(3);
  SCHED();
  u32x4 yr[2], yi[2];
  float g;
  const int t = 32 * (mt >> 1) + arow + 4 * (mt & 1);
  f32x4 oc[4];
#pragma unroll
  for (int n = 0; n < 4; ++n) oc[n] = (f32x4){0, 0, 0, 0};
#pragma unroll
  for (int c = 0; c < 7; ++c) {
    if (c < 3) ldh(c + 4);
    if (c == 3) {
#pragma unroll
      for (int k = 0; k < 2; ++k) {
        yr[k] = __builtin_amdgcn_raw_buffer_load_b128(rws, lane * 16 + k * 1024, o2 + 2 * dg * 1024, 16);
        yi[k] = __builtin_amdgcn_raw_buffer_load_b128(rws, lane * 16 + k * 1024, o2 + (14 + 2 * dg) * 1024, 16);
      }
      g = __hip_atomic_load((GAS float*)(ws + OFF_G) + t, RLX_AGENT);
    }
    SCHED();
    if (c < 6) rdw(c + 1);
#pragma unroll
    for (int i = 0; i < 16; ++i) oc[i & 3] = MFMA(CAT(wl[c & 1][i], wh[c & 1][i]), *(const bf16x8*)&hf[c][i >> 2], oc[i & 3]);
    if (c < 6) {
#pragma unroll
      for (int i = 0; i < 16; ++i) { SGB(0x8, 1); SGB(0x100, 2); }
    }
    SCHED();
  }
  STAMP(15);
  const float g1 = 1.0f - g;
#pragma unroll
  for (int n = 0; n < 4; ++n) {
    const unsigned r0 = yr[n >> 1][(n & 1) * 2], r1 = yr[n >> 1][(n & 1) * 2 + 1];
    const unsigned i0 = yi[n >> 1][(n & 1) * 2], i1 = yi[n >> 1][(n & 1) * 2 + 1];
    const f32x4 y = {bflo(r0) - bflo(i0), bfhi(r0) - bfhi(i0), bflo(r1) - bflo(i1), bfhi(r1) - bfhi(i1)};
    const f32x4 v = y * g + oc[n] * g1;
    *(u32x2*)(out + (size_t)t * D + 64 * dg + 16 * n + 4 * fq) = (u32x2){pk_bf16(v[0], v[1]), pk_bf16(v[2], v[3])};
  }
  STAMP(16);
  STAMP_FLUSH();
}

extern "C" void kernel_launch(void* const* d_in, const int* in_sizes, int n_in, void* d_out, int out_size, void* d_ws,
                              size_t ws_size, hipStream_t stream) {
  fused<<<256, 512, 0, stream>>>((const u16*)d_in[0], (const u16*)d_in[1], (const u16*)d_in[2], (const u16*)d_in[3],
                                 (const u16*)d_in[4], (const u16*)d_in[6], (const u16*)d_in[7], (const u16*)d_in[8],
                                 (const u16*)d_in[9], (const u16*)d_in[10], (char*)d_ws, (u16*)d_out);
}
```

```cpp
#include <hip/hip_runtime.h>
#include <hip/hip_bf16.h>

typedef unsigned short u16;
typedef unsigned long long u64;
typedef short bf16x8 __attribute__((ext_vector_type(8)));
typedef short bf16x4 __attribute__((ext_vector_type(4)));
typedef float f32x2 __attribute__((ext_vector_type(2)));
typedef float f32x4 __attribute__((ext_vector_type(4)));
typedef unsigned u32x2 __attribute__((ext_vector_type(2)));
typedef unsigned u32x4 __attribute__((ext_vector_type(4)));
#define LAS __attribute__((address_space(3)))
#define GAS __attribute__((address_space(1)))
#define RLX_AGENT __ATOMIC_RELAXED, __HIP_MEMORY_SCOPE_AGENT

#define WAITV(n) asm volatile("s_waitcnt vmcnt(%0)" ::"n"(n) : "memory")
#define WAITL0() asm volatile("s_waitcnt lgkmcnt(0)" ::: "memory")
#define BAR() do { asm volatile("" ::: "memory"); __builtin_amdgcn_s_barrier(); asm volatile("" ::: "memory"); } while (0)
#define SCHED() __builtin_amdgcn_sched_barrier(0)
#define SGB(mask, n) __builtin_amdgcn_sched_group_barrier(mask, n, 0)
#define MFMA(a, b, c) __builtin_amdgcn_mfma_f32_16x16x32_bf16(a, b, c, 0, 0, 0)
#define TRD(p) __builtin_amdgcn_ds_read_tr16_b64_v4i16((LAS bf16x4*)(p))
#define CAT(lo, hi) __builtin_shufflevector(lo, hi, 0, 1, 2, 3, 4, 5, 6, 7)

template <bool SC1 = false>
__device__ __forceinline__ void glds(const void* sbase, unsigned voff, unsigned l) {
  unsigned keep;
  l = __builtin_amdgcn_readfirstlane(l);
  if (SC1)
    asm volatile("s_mov_b32 %0, m0\n\ts_mov_b32 m0, %3\n\ts_nop 0\n\tglobal_load_lds_dwordx4 %1, %2 sc1\n\ts_mov_b32 m0, %0"
                 : "=&s"(keep) : "v"(voff), "s"(sbase), "s"(l) : "memory");
  else
    asm volatile("s_mov_b32 %0, m0\n\ts_mov_b32 m0, %3\n\ts_nop 0\n\tglobal_load_lds_dwordx4 %1, %2\n\ts_mov_b32 m0, %0"
                 : "=&s"(keep) : "v"(voff), "s"(sbase), "s"(l) : "memory");
}
__device__ __forceinline__ void touch(const void* sbase, unsigned voff, unsigned l) {
  unsigned keep;
  l = __builtin_amdgcn_readfirstlane(l);
  asm volatile("s_mov_b32 %0, m0\n\ts_mov_b32 m0, %3\n\ts_nop 0\n\tglobal_load_lds_dword %1, %2\n\ts_mov_b32 m0, %0"
               : "=&s"(keep) : "v"(voff), "s"(sbase), "s"(l) : "memory");
}

__device__ __forceinline__ void touch64(const void* p, unsigned l) {
  unsigned keep;
  l = __builtin_amdgcn_readfirstlane(l);
  asm volatile("s_mov_b32 %0, m0\n\ts_mov_b32 m0, %2\n\ts_nop 0\n\tglobal_load_lds_dword %1, off\n\ts_mov_b32 m0, %0"
               : "=&s"(keep) : "v"(p), "s"(l) : "memory");
}
#define TLB_SHIFT 14

constexpr int S = 2048, D = 448;

__device__ __forceinline__ float bf2f(u16 v) { return __uint_as_float(((unsigned)v) << 16); }
__device__ __forceinline__ float bflo(unsigned v) { return __uint_as_float(v << 16); }
__device__ __forceinline__ float bfhi(unsigned v) { return __uint_as_float(v & 0xffff0000u); }
__device__ __forceinline__ unsigned pk_bf16(float lo, float hi) {
  typedef __bf16 bf2 __attribute__((ext_vector_type(2)));
  f32x2 v = {lo, hi};
  bf2 b = __builtin_convertvector(v, bf2);
  return *(unsigned*)&b;
}
__device__ __forceinline__ f32x2 gelu2(f32x2 v) {
  const float c0 = -2.0f * 0.7978845608028654f * 1.4426950408889634f;
  const float c1 = c0 * 0.044715f;
  const f32x2 u = v * ((v * v) * c1 + c0);
  f32x2 e = {__builtin_amdgcn_exp2f(u.x), __builtin_amdgcn_exp2f(u.y)};
  e = e + 1.0f;
  const f32x2 rc = {__builtin_amdgcn_rcpf(e.x), __builtin_amdgcn_rcpf(e.y)};
  return v * rc;
}
__device__ __forceinline__ f32x4 gelu4(f32x4 v) {
  const f32x2 lo = gelu2((f32x2){v[0], v[1]}), hi = gelu2((f32x2){v[2], v[3]});
  return (f32x4){lo.x, lo.y, hi.x, hi.y};
}
__device__ __forceinline__ float xsum(float s) {
  auto a = __builtin_amdgcn_permlane16_swap(__float_as_uint(s), __float_as_uint(s), false, false);
  s = __uint_as_float(a[0]) + __uint_as_float(a[1]);
  auto b = __builtin_amdgcn_permlane32_swap(__float_as_uint(s), __float_as_uint(s), false, false);
  return __uint_as_float(b[0]) + __uint_as_float(b[1]);
}

constexpr unsigned OFF_BT = 0;
constexpr unsigned OFF_O2 = OFF_BT + 1792 * S * 2;
constexpr unsigned OFF_G = OFF_O2 + 1792 * S * 2;
constexpr unsigned OFF_X = OFF_G + 8192;
constexpr unsigned OFF_F1 = OFF_X + 64 * 8 * 512;
constexpr unsigned OFF_F2 = OFF_F1 + 8192;
constexpr unsigned OFF_E = OFF_F2 + 8192;
constexpr unsigned WS_USED = OFF_E + 128;
constexpr u64 EPOCH_STEP = 0x9E3779B97F4A7C15ull;
constexpr unsigned SPIN_CAP = 1u << 20;

#define STAMP_DECL
#define STAMP(i)
#define STAMP_FLUSH()

constexpr unsigned WR0 = 114688, WSL = 14336;
constexpr int STA = 16384, STB = 14336, STG = STA + STB, RING = 4, NT = 32, LEAD = 4;
constexpr unsigned SPARE = RING * STG, JUNK = SPARE + 32768;
__device__ __forceinline__ constexpr unsigned wo_loc(int c) { return c < 2 ? SPARE + c * 16384 : (c - 2) * 16384; }

__device__ __forceinline__ void wait_flags(GAS u64* p, u64 tag) {
  for (unsigned spins = 0; spins < SPIN_CAP; ++spins) {
    const u64 v = __hip_atomic_load(p, RLX_AGENT);
    if (__all(v == tag)) break;
    __builtin_amdgcn_s_sleep(1);
  }
  __builtin_amdgcn_fence(__ATOMIC_ACQUIRE, "wavefront");
}

struct Loader2 {
  const char* base; unsigned step, l0; int cls, nxt, slot;
  template <int P, bool SC1> __device__ __forceinline__ void issue(const unsigned (&off)[P]) {
    const int st = ((nxt & 3) == cls) ? ((nxt + LEAD) & (NT - 1)) : nxt;
    const char* g = base + (size_t)st * step;
    const unsigned l = l0 + slot * STG;
#pragma unroll
    for (int i = 0; i < P; ++i) glds<SC1>(g, off[i], l + i * 1024);
    ++nxt;
    slot = (slot + 1) & (RING - 1);
  }
};

__global__ __launch_bounds__(512, 2) void fused(const u16* __restrict__ x, const u16* __restrict__ wsr, const u16* __restrict__ wsi,
                                                const u16* __restrict__ wdr, const u16* __restrict__ wdi, const u16* __restrict__ win,
                                                const u16* __restrict__ wsp, const u16* __restrict__ bsp, const u16* __restrict__ wout,
                                                const u16* __restrict__ gw, char* __restrict__ ws, u16* __restrict__ out) {
  __shared__ __attribute__((aligned(1024))) char smem[163840];
  LAS char* lds = (LAS char*)smem;
  const int wid = __builtin_amdgcn_readfirstlane(threadIdx.x >> 6), lane = threadIdx.x & 63;
  STAMP_DECL; STAMP(0);
  const int b = blockIdx.x, xcd = b & 7, r = b >> 3;
  int mi, p, cls;
  if (xcd < 4) { mi = xcd * 4 + (r & 3); p = 8 + (r >> 2); cls = ((r >> 2) + r) & 3; }
  else { const int h = xcd - 4; mi = (h & 1) * 8 + (r & 7); p = (h >> 1) * 4 + (r >> 3); cls = ((r >> 3) + r) & 3; }
  const bool G = xcd < 4;
  const int N1 = G ? 14 : 7;
  const u64 ord = (u64)(cls == 0 ? 0x5463120u : cls == 1 ? 0x6532410u : cls == 2 ? 0x6542031u : 0x6423501u) * 0x10000001ull;
  const int rg = 4 * xcd + (r & 3), dg = r >> 2;
  u64 tag = *(const __attribute__((address_space(4))) u64*)(ws + OFF_E);
  const unsigned ldsb = __builtin_amdgcn_readfirstlane((unsigned)(size_t)lds);
  const unsigned l3 = lane >> 3, l7 = lane & 7, l4 = (lane >> 4) & 1;

  if (wid >= 4) {
    const int lw = wid - 4;
    const unsigned wo_off = l3 * 896 + (((((l7 >> 1) ^ ((lane >> 4) & 3)) << 1) | (l7 & 1)) << 4);
    const char* wo_base = (const char*)wout + (dg < 7 ? dg : 0) * 128 + (size_t)lw * 32 * 896;
    auto wo_chunk = [&](int c) {
      int cc = c + dg;
      if (cc >= 7) cc -= 7;
#pragma unroll
      for (int i = 0; i < 4; ++i) glds(wo_base + (size_t)(cc * 128 + i * 8) * 896, wo_off, ldsb + wo_loc(c) + lw * 4096 + i * 1024);
    };
    const unsigned warm_off = (unsigned)((r * 4 + lw) * 49 + (lane < 49 ? lane : 48)) * 128;
    if (lw < 2) {
      unsigned off[8];
      {
        const char* xg = (const char*)x + (size_t)(mi * 128 + lw * 64) * 896;
#pragma unroll
        for (int i = 0; i < 8; ++i) off[i] = (8 * i + l3) * 896 + ((l7 ^ (l4 | ((i & 3) << 1))) << 4);
        auto issue = [&](int s) {
          const unsigned st = (ord >> (4 * s)) & 7;
#pragma unroll
          for (int i = 0; i < 8; ++i) glds(xg + st * 128, off[i], ldsb + st * 16384 + lw * 8192 + i * 1024);
        };
        issue(0);
        issue(1);
        asm volatile("" : "+s"(tag));
        STAMP(1);
        WAITV(8);
        BAR();
        issue(2);
#pragma unroll 1
        for (int s = 0; s < 4; ++s) { WAITV(8); BAR(); issue(s + 3); }
        WAITV(8); BAR();
        WAITV(0);
#pragma unroll 1
        for (int s = 5; s < N1; ++s) BAR();
      }
      STAMP(2);
      const u16* am = p < 4 ? wsr : wsi;
      am = G ? wsp : am;
#pragma unroll
      for (int i = 0; i < 8; ++i) off[i] = (8 * i + l3) * 4096 + ((l7 ^ (l4 | ((i & 3) << 1))) << 4);
      Loader2 L{(const char*)am + (size_t)(mi * 128 + lw * 64) * 4096, 128, ldsb + lw * 8192, cls, 0, 0};
      L.issue<8, false>(off);
      L.issue<8, false>(off);
      if (!G) {
        const int row0 = mi * 128 + p * 16 + lw * 8;
        const int lc = lane < 56 ? lane : 55;
        const u32x4 gv = *(const u32x4*)(gw + lc * 8);
        u32x4 xv[8];
#pragma unroll
        for (int i = 0; i < 8; ++i) xv[i] = *(const u32x4*)(x + (size_t)(row0 + i) * D + lc * 8);
        float res = 0.f;
#pragma unroll
        for (int i = 0; i < 8; ++i) {
          float a = 0.f;
#pragma unroll
          for (int j = 0; j < 4; ++j) a += bflo(xv[i][j]) * bflo(gv[j]) + bfhi(xv[i][j]) * bfhi(gv[j]);
          a = lane < 56 ? a : 0.f;
#pragma unroll
          for (int o = 8; o >= 1; o >>= 1) a += __shfl_xor(a, o);
          a = xsum(a);
          if (lane == i) res = a;
        }
        if (lane < 8) __hip_atomic_store((GAS float*)(ws + OFF_G) + row0 + lane, 1.0f / (1.0f + __expf(-res)), RLX_AGENT);
      }
      L.issue<8, false>(off);
      L.issue<8, false>(off);
      WAITV(24);
      STAMP(3);
      BAR();
#pragma unroll 1
      for (int t = 0; t < NT - 4; ++t) {
        WAITV(16);
        BAR();
        if (L.nxt == 20 + cls) touch(wout, warm_off, ldsb + JUNK);
        L.issue<8, false>(off);
      }
      wo_chunk(0); wo_chunk(1);
      WAITV(24); BAR();
      wo_chunk(2);
      WAITV(20); BAR();
      wo_chunk(3); wo_chunk(4);
      WAITV(20); BAR();
    } else {
      const int l2 = lw - 2;
      unsigned off[7];
      {
        const char* wb; unsigned step;
        if (G) {
          wb = (const char*)win + (p - 8) * 224 + 1792; step = 64 * 3584;
#pragma unroll
          for (int i = 0; i < 7; ++i) {
            const unsigned u = (l2 * 7 + i) * 64 + lane, R = u / 14, c = u % 14;
            off[i] = (R ^ (((R >> 3) & 1) << 2)) * 3584 + c * 16;
          }
        } else {
          const u16* dm = p < 4 ? wdr : wdi;
          wb = (const char*)dm + (size_t)(p & 3) * 112 * 896; step = 128;
#pragma unroll
          for (int i = 0; i < 7; ++i) {
            const unsigned lr = 8 * (l2 * 7 + i) + l3;
            off[i] = lr * 896 + ((l7 ^ ((lr >> 1) & 7)) << 4);
          }
        }
        int slot = 0;
        auto issue = [&](int s) {
          const unsigned st = (ord >> (4 * s)) & 7;
          const char* g = wb + st * step - (s >= 7 ? 1792 : 0);
#pragma unroll
          for (int i = 0; i < 7; ++i) glds(g, off[i], ldsb + WR0 + slot * WSL + l2 * 7168 + i * 1024);
          slot = slot == 2 ? 0 : slot + 1;
        };
        issue(0);
        issue(1);
        asm volatile("" : "+s"(tag));
        STAMP(1);
        WAITV(7);
        BAR();
        issue(2);
#pragma unroll 1
        for (int s = 0; s < N1 - 3; ++s) { WAITV(7); BAR(); issue(s + 3); }
        WAITV(7); BAR();
        WAITV(0); BAR();
        BAR();
      }
      STAMP(2);
      tag += EPOCH_STEP;
#pragma unroll
      for (int i = 0; i < 7; ++i) off[i] = i * 1024 + lane * 16;
      Loader2 L{ws + OFF_BT + (size_t)p * 64 * 7 * 1024 + l2 * 7168, STB, ldsb + STA + l2 * 7168, cls, 0, 0};
      wait_flags((GAS u64*)(ws + OFF_F1) + p * 64 + lane, tag);
      STAMP(4);
      L.issue<7, true>(off);
      L.issue<7, true>(off);
      WAITV(7);
      STAMP(3);
      BAR();
      L.issue<7, true>(off);
      L.issue<7, true>(off);
#pragma unroll 1
      for (int t = 0; t < NT - 4; ++t) {
        WAITV(14);
        BAR();
        if (L.nxt == 20 + cls) touch(wout, warm_off, ldsb + JUNK);
        L.issue<7, true>(off);
      }
      wo_chunk(0); wo_chunk(1);
      WAITV(22); BAR();
      wo_chunk(2);
      WAITV(19); BAR();
      wo_chunk(3); wo_chunk(4);
      WAITV(20); BAR();
    }
    wo_chunk(5); wo_chunk(6);
    WAITV(0);
    STAMP(5);
    BAR();
    STAMP_FLUSH();
    return;
  }

  if (wid == 0) {
    const u16* am = p < 4 ? wsr : wsi;
    am = G ? wsp : am;
    const char* a0 = (const char*)am + (size_t)(G ? xcd * 512 : (xcd & 1) * 1024) * 4096;
    constexpr unsigned n0 = (4u << 20) >> TLB_SHIFT, n1 = n0 + (WS_USED >> TLB_SHIFT) + 1, n2 = n1 + ((S * D * 2) >> TLB_SHIFT) + 1,
                       n3 = n2 + ((896 * D * 2) >> TLB_SHIFT) + 1;
    const unsigned i = r * 64 + lane;
    const char* q = (const char*)gw;
    if (i < n0) q = a0 + (((size_t)i << TLB_SHIFT) & (G ? (2u << 20) - 1 : (4u << 20) - 1));
    else if (i < n1) q = ws + min((i - n0) << TLB_SHIFT, WS_USED - 4);
    else if (i < n2) q = (const char*)out + min((i - n1) << TLB_SHIFT, (unsigned)(S * D * 2 - 4));
    else if (i < n3) q = (const char*)wout + min((i - n2) << TLB_SHIFT, (unsigned)(896 * D * 2 - 4));
    touch64(q, ldsb + 163584);
  }
  const int fi = lane & 15, fq = lane >> 4;
  const unsigned arow = 8 * (fi >> 2) + (fi & 3);
  const unsigned tq = fi >> 2, tp = lane & 3;
  const unsigned aoff0 = (32 * wid + arow) * 128 + ((fq ^ (fi >> 1)) << 4);
  const unsigned aoff1 = (32 * wid + arow) * 128 + (((fq + 4) ^ (fi >> 1)) << 4);
  const __amdgpu_buffer_rsrc_t rws = __builtin_amdgcn_make_buffer_rsrc(ws, 0, WS_USED, 0x00020000);
  float bb[2] = {bf2f(bsp[mi * 128 + 32 * wid + arow]), bf2f(bsp[mi * 128 + 32 * wid + arow + 4])};
  asm volatile("" : "+v"(bb[0]), "+v"(bb[1]), "+s"(tag));
  tag += EPOCH_STEP;
  f32x4 acc[2][7];
  auto zero_acc = [&]() {
#pragma unroll
    for (int m = 0; m < 2; ++m)
#pragma unroll
      for (int n = 0; n < 7; ++n) { acc[m][n] = (f32x4){0, 0, 0, 0}; asm volatile("" : "+v"(acc[m][n])); }
  };
  zero_acc();
  u32x2 z1p[2][7];
  const unsigned bt_off = OFF_BT + ((p * 64 + 4 * mi + wid) * 7) * 1024;
  GAS u64* f1 = (GAS u64*)(ws + OFF_F1) + p * 64 + 4 * mi + wid;
  if (G) {
    bf16x8 fa[2][2];
    bf16x4 fl[2][7], fh[2][7];
    const unsigned b0 = WR0 + (8 * fq + 4 * (fq & 1) + tq) * 224 + tp * 8;
    const unsigned b1 = WR0 + (8 * fq + 4 * (1 - (fq & 1)) + tq) * 224 + tp * 8;
    int slot = 0;
    auto rd = [&](int k, int s, int sl) {
      LAS char* lx = lds + ((ord >> (4 * s)) & 7) * 16384 + (k ? aoff1 : aoff0);
      LAS char* lw = lds + sl * WSL + k * 7168;
#pragma unroll
      for (int h = 0; h < 2; ++h) fa[k][h] = *(const LAS bf16x8*)(lx + h * 512);
#pragma unroll
      for (int n = 0; n < 7; ++n) { fl[k][n] = TRD(lw + b0 + n * 32); fh[k][n] = TRD(lw + b1 + n * 32); }
    };
    auto mm = [&](int k) {
#pragma unroll
      for (int n = 0; n < 7; ++n)
#pragma unroll
        for (int h = 0; h < 2; ++h) acc[h][n] = MFMA(CAT(fl[k][n], fh[k][n]), fa[k][h], acc[h][n]);
      SGB(0x8, 1); SGB(0x100, 2); SGB(0x8, 1); SGB(0x100, 2);
#pragma unroll
      for (int i = 0; i < 12; ++i) { SGB(0x8, 1); SGB(0x100, 1); }
      SCHED();
    };
    auto run = [&](int s0, int s1) {
#pragma unroll 1
      for (int s = s0; s < s1; ++s) {
        rd(1, s, slot); mm(0);
        slot = slot == 2 ? 0 : slot + 1;
        WAITL0(); BAR();
        rd(0, s + 1, slot); mm(1);
      }
    };
    BAR();
    STAMP(1);
    rd(0, 0, 0);
    SCHED();
    run(0, 7);
    STAMP(2);
    f32x4 z2[2][7];
    GAS u64* xch = (GAS u64*)(ws + OFF_X) + (mi * 4 + wid) * 8 * 64 + lane;
    const u64 tag40 = tag << 24;
    {
      float mean_p[2], m2_p[2];
#pragma unroll
      for (int h = 0; h < 2; ++h) {
        f32x4 s4 = {0, 0, 0, 0};
#pragma unroll
        for (int n = 0; n < 7; ++n) { z2[h][n] = gelu4(acc[h][n]); s4 += z2[h][n]; }
        mean_p[h] = xsum((s4[0] + s4[1]) + (s4[2] + s4[3])) * (1.0f / 112.0f);
      }
#pragma unroll
      for (int h = 0; h < 2; ++h) {
        f32x4 q4 = {0, 0, 0, 0};
#pragma unroll
        for (int n = 0; n < 7; ++n) { const f32x4 d = z2[h][n] - mean_p[h]; q4 += d * d; }
        m2_p[h] = xsum((q4[0] + q4[1]) + (q4[2] + q4[3]));
      }
      const float val = (fq & 2) ? ((fq & 1) ? m2_p[1] : m2_p[0]) : ((fq & 1) ? mean_p[1] : mean_p[0]);
      const unsigned bits = (__float_as_uint(val) + 0x80u) >> 8;
      __hip_atomic_store(xch + (p - 8) * 64, tag40 | bits, RLX_AGENT);
    }
    zero_acc();
    STAMP(3);
    run(7, 10);
    u64 gr[8];
#pragma unroll
    for (int c = 0; c < 8; ++c) gr[c] = __hip_atomic_load(xch + c * 64, RLX_AGENT);
    SCHED();
    run(10, 11);
    STAMP(4);
    for (unsigned spins = 0;; ++spins) {
      unsigned bh = 0, bl = 0;
#pragma unroll
      for (int c = 0; c < 8; ++c) {
        bh |= (unsigned)(gr[c] >> 32) ^ (unsigned)(tag40 >> 32);
        bl |= (unsigned)gr[c] ^ (unsigned)tag40;
      }
      if (__all((bh | (bl >> 24)) == 0) || spins > SPIN_CAP) break;
#pragma unroll
      for (int c = 0; c < 8; ++c) gr[c] = __hip_atomic_load(xch + c * 64, RLX_AGENT);
    }
    STAMP(5);
    {
      float pv[8], sum = 0.f, dev = 0.f;
#pragma unroll
      for (int c = 0; c < 8; ++c) { pv[c] = __uint_as_float((unsigned)gr[c] << 8); sum += pv[c]; }
      const float mean = sum * 0.125f;
#pragma unroll
      for (int c = 0; c < 8; ++c) { const float d = pv[c] - mean; dev = __builtin_fmaf(d, d, dev); }
      const float mine = (fq & 2) ? sum : dev * 112.0f;
      auto sm = __builtin_amdgcn_permlane32_swap(__float_as_uint(mine), __float_as_uint(mine), false, false);
      const float rstd = __builtin_amdgcn_rsqf((__uint_as_float(sm[0]) + __uint_as_float(sm[1])) * (1.0f / 896.0f) + 1e-5f);
      auto ml = __builtin_amdgcn_permlane32_swap(__float_as_uint(mean), __float_as_uint(mean), false, false);
      const float nm = -__uint_as_float(ml[0]) * rstd;
      auto rs = __builtin_amdgcn_permlane16_swap(__float_as_uint(rstd), __float_as_uint(rstd), false, false);
      auto mu = __builtin_amdgcn_permlane16_swap(__float_as_uint(nm), __float_as_uint(nm), false, false);
      LAS char* sc = lds + ((ord >> (4 * (wid >> 1))) & 7) * 16384 + (wid & 1) * 8192;
      const unsigned wsw = ((fi & 7) << 2) ^ (fi >> 2);
#pragma unroll
      for (int h = 0; h < 2; ++h)
#pragma unroll
        for (int n = 0; n < 7; ++n) {
          const f32x4 v = z2[h][n] * __uint_as_float(rs[h]) + __uint_as_float(mu[h]);
          const u32x2 o = {pk_bf16(v[0], v[1]), pk_bf16(v[2], v[3])};
          *(LAS u32x2*)(sc + (arow + 4 * h) * 256 + (((4 * n + fq) ^ wsw) << 3)) = o;
        }
      WAITL0();
      const unsigned ri = (fq << 2) | tq, rsw = ((ri & 7) << 2) ^ (ri >> 2);
#pragma unroll
      for (int n = 0; n < 7; ++n) {
        const unsigned a = (8 * fq + tq) * 256 + (((4 * n + tp) ^ rsw) << 3);
        const bf16x8 o = CAT(TRD(sc + a), TRD(sc + a + 1024));
        __builtin_amdgcn_raw_buffer_store_b128(*(const u32x4*)&o, rws, lane * 16, bt_off + n * 1024, 16);
      }
    }
    SCHED();
    STAMP(6);
    run(11, 13);
    WAITV(0);
    if (lane == 0) __hip_atomic_store(f1, tag, RLX_AGENT);
    STAMP(7);
    run(13, 14);
    STAMP(8);
#pragma unroll
    for (int h = 0; h < 2; ++h)
#pragma unroll
      for (int n = 0; n < 7; ++n) {
        const f32x4 v = gelu4(acc[h][n]);
        z1p[h][n] = (u32x2){pk_bf16(v[0], v[1]), pk_bf16(v[2], v[3])};
      }
  } else {
    bf16x8 fa[2][2], fb[2][7];
    const unsigned b0 = WR0 + fi * 128 + ((fq ^ (fi >> 1)) << 4);
    const unsigned b1 = WR0 + fi * 128 + (((fq + 4) ^ (fi >> 1)) << 4);
    int slot = 0;
    auto rd = [&](int k, int s, int sl) {
      LAS char* lx = lds + ((ord >> (4 * s)) & 7) * 16384 + (k ? aoff1 : aoff0);
      LAS char* lw = lds + sl * WSL + (k ? b1 : b0);
#pragma unroll
      for (int h = 0; h < 2; ++h) fa[k][h] = *(const LAS bf16x8*)(lx + h * 512);
#pragma unroll
      for (int n = 0; n < 7; ++n) fb[k][n] = *(const LAS bf16x8*)(lw + n * 2048);
    };
    auto mm = [&](int k) {
#pragma unroll
      for (int n = 0; n < 7; ++n)
#pragma unroll
        for (int h = 0; h < 2; ++h) acc[h][n] = MFMA(fa[k][h], fb[k][n], acc[h][n]);
#pragma unroll
      for (int i = 0; i < 9; ++i) { SGB(0x8, 1); SGB(0x100, 1); }
      SGB(0x8, 5);
      SCHED();
    };
    BAR();
    STAMP(1);
    rd(0, 0, 0);
    SCHED();
#pragma unroll 1
    for (int s = 0; s < 7; ++s) {
      rd(1, s, slot); mm(0);
      slot = slot == 2 ? 0 : slot + 1;
      WAITL0(); BAR();
      rd(0, s + 1, slot); mm(1);
    }
    STAMP(2);
#pragma unroll
    for (int n = 0; n < 7; ++n) {
      const u32x4 o = {pk_bf16(acc[0][n][0], acc[0][n][1]), pk_bf16(acc[0][n][2], acc[0][n][3]),
                       pk_bf16(acc[1][n][0], acc[1][n][1]), pk_bf16(acc[1][n][2], acc[1][n][3])};
      __builtin_amdgcn_raw_buffer_store_b128(o, rws, lane * 16, bt_off + n * 1024, 16);
    }
    STAMP(6);
    WAITV(0);
    if (lane == 0) __hip_atomic_store(f1, tag, RLX_AGENT);
    STAMP(7);
#pragma unroll
    for (int h = 0; h < 2; ++h)
#pragma unroll
      for (int n = 0; n < 7; ++n) z1p[h][n] = (u32x2){0x3f803f80u, 0x3f803f80u};
    bb[0] = bb[1] = 0.f;
  }
  zero_acc();
  {
    const unsigned boff = STA + lane * 16;
    bf16x8 fa[2][2][2], fb[2][2][7];
    auto rd = [&](int set, int slot) {
      LAS char* l = lds + slot * STG;
#pragma unroll
      for (int k = 0; k < 2; ++k) {
#pragma unroll
        for (int m = 0; m < 2; ++m) fa[set][k][m] = *(const LAS bf16x8*)(l + (k ? aoff1 : aoff0) + m * 512);
#pragma unroll
        for (int n = 0; n < 7; ++n) fb[set][k][n] = *(const LAS bf16x8*)(l + boff + k * 7168 + n * 1024);
      }
    };
    auto mm = [&](int set) {
#pragma unroll
      for (int k = 0; k < 2; ++k)
#pragma unroll
        for (int n = 0; n < 7; ++n)
#pragma unroll
          for (int m = 0; m < 2; ++m) acc[m][n] = MFMA(fb[set][k][n], fa[set][k][m], acc[m][n]);
    };
    auto sched = [&]() {
#pragma unroll
      for (int i = 0; i < 18; ++i) { SGB(0x8, 1); SGB(0x100, 1); }
      SGB(0x8, 10);
      SCHED();
    };
    STAMP(9);
    WAITL0();
    BAR();
    STAMP(10);
    rd(0, 0);
    int slot = 1;
#pragma unroll 1
    for (int t = 0; t < NT - 2; t += 2) {
      WAITL0(); BAR();
      rd(1, slot); mm(0); sched();
      slot = (slot + 1) & (RING - 1);
      WAITL0(); BAR();
      rd(0, slot); mm(1); sched();
      slot = (slot + 1) & (RING - 1);
    }
    WAITL0(); BAR();
    rd(1, slot); mm(0); sched();
    mm(1);
    SCHED();
  }
  STAMP(11);
  {
    const int odd = p & 1;
    u32x2 o[2][7];
#pragma unroll
    for (int m = 0; m < 2; ++m)
#pragma unroll
      for (int n = 0; n < 7; ++n) {
        const f32x4 z = {bflo(z1p[m][n][0]), bfhi(z1p[m][n][0]), bflo(z1p[m][n][1]), bfhi(z1p[m][n][1])};
        const f32x4 v = (acc[m][n] + bb[m]) * z;
        o[m][n] = (u32x2){pk_bf16(v[0], v[1]), pk_bf16(v[2], v[3])};
      }
    const unsigned o2 = OFF_O2 + ((mi * 8 + wid * 2) * 56 + ((7 * p) >> 1)) * 1024;
#pragma unroll
    for (int m = 0; m < 2; ++m) {
      const unsigned om = o2 + m * 56 * 1024;
      if (odd) {
        __builtin_amdgcn_raw_buffer_store_b64(o[m][0], rws, lane * 16 + 8, om, 16);
#pragma unroll
        for (int k = 0; k < 3; ++k)
          __builtin_amdgcn_raw_buffer_store_b128((u32x4){o[m][2 * k + 1][0], o[m][2 * k + 1][1], o[m][2 * k + 2][0], o[m][2 * k + 2][1]},
                                                 rws, lane * 16, om + (k + 1) * 1024, 16);
      } else {
#pragma unroll
        for (int k = 0; k < 3; ++k)
          __builtin_amdgcn_raw_buffer_store_b128((u32x4){o[m][2 * k][0], o[m][2 * k][1], o[m][2 * k + 1][0], o[m][2 * k + 1][1]},
                                                 rws, lane * 16, om + k * 1024, 16);
        __builtin_amdgcn_raw_buffer_store_b64(o[m][6], rws, lane * 16, om + 3 * 1024, 16);
      }
    }
  }
  STAMP(12);
  WAITV(0);
  if (lane == 0) __hip_atomic_store((GAS u64*)(ws + OFF_F2) + (4 * mi + wid) * 16 + p, tag, RLX_AGENT);
  STAMP(13);
  BAR();
  if (r >= 28) { STAMP_FLUSH(); return; }
  const int mt = 4 * rg + wid;
  unsigned wb[4];
#pragma unroll
  for (int n = 0; n < 4; ++n) wb[n] = (4 * fq + tq) * 128 + ((n ^ (((4 * fq + tq) >> 1) & 3)) << 5) + tp * 8;
  bf16x4 wl[2][16], wh[2][16];
  auto rdw = [&](int c) {
#pragma unroll
    for (int i = 0; i < 16; ++i) {
      LAS char* a = lds + wb[i & 3] + (wo_loc(c) + (i >> 2) * 4096);
      wl[c & 1][i] = TRD(a);
      wh[c & 1][i] = TRD(a + 2048);
    }
  };
  rdw(0);
  SCHED();
  wait_flags((GAS u64*)(ws + OFF_F2) + (mt >> 1) * 16 + fi, tag);
  STAMP(14);
  if (b == 0 && threadIdx.x == 0) __hip_atomic_store((GAS u64*)(ws + OFF_E), tag, RLX_AGENT);
  const unsigned o2 = OFF_O2 + mt * 56 * 1024;
  u32x4 hf[7][4];
  auto ldh = [&](int c) {
    int cc = c + dg;
    if (cc >= 7) cc -= 7;
#pragma unroll
    for (int k = 0; k < 4; ++k) hf[c][k] = __builtin_amdgcn_raw_buffer_load_b128(rws, lane * 16 + k * 1024, o2 + (28 + cc * 4) * 1024, 16);
  };
  ldh(0); ldh(1); ldh(2); ldh(3);
  SCHED();
  u32x4 yr[2], yi[2];
  float g;
  const int t = 32 * (mt >> 1) + arow + 4 * (mt & 1);
  f32x4 oc[4];
#pragma unroll
  for (int n = 0; n < 4; ++n) oc[n] = (f32x4){0, 0, 0, 0};
#pragma unroll
  for (int c = 0; c < 7; ++c) {
    if (c < 3) ldh(c + 4);
    if (c == 3) {
#pragma unroll
      for (int k = 0; k < 2; ++k) {
        yr[k] = __builtin_amdgcn_raw_buffer_load_b128(rws, lane * 16 + k * 1024, o2 + 2 * dg * 1024, 16);
        yi[k] = __builtin_amdgcn_raw_buffer_load_b128(rws, lane * 16 + k * 1024, o2 + (14 + 2 * dg) * 1024, 16);
      }
      g = __hip_atomic_load((GAS float*)(ws + OFF_G) + t, RLX_AGENT);
    }
    SCHED();
    if (c < 6) rdw(c + 1);
#pragma unroll
    for (int i = 0; i < 16; ++i) oc[i & 3] = MFMA(CAT(wl[c & 1][i], wh[c & 1][i]), *(const bf16x8*)&hf[c][i >> 2], oc[i & 3]);
    if (c < 6) {
#pragma unroll
      for (int i = 0; i < 16; ++i) { SGB(0x8, 1); SGB(0x100, 2); }
    }
    SCHED();
  }
  STAMP(15);
  const float g1 = 1.0f - g;
#pragma unroll
  for (int n = 0; n < 4; ++n) {
    const unsigned r0 = yr[n >> 1][(n & 1) * 2], r1 = yr[n >> 1][(n & 1) * 2 + 1];
    const unsigned i0 = yi[n >> 1][(n & 1) * 2], i1 = yi[n >> 1][(n & 1) * 2 + 1];
    const f32x4 y = {bflo(r0) - bflo(i0), bfhi(r0) - bfhi(i0), bflo(r1) - bflo(i1), bfhi(r1) - bfhi(i1)};
    const f32x4 v = y * g + oc[n] * g1;
    *(u32x2*)(out + (size_t)t * D + 64 * dg + 16 * n + 4 * fq) = (u32x2){pk_bf16(v[0], v[1]), pk_bf16(v[2], v[3])};
  }
  STAMP(16);
  STAMP_FLUSH();
}

extern "C" void kernel_launch(void* const* d_in, const int* in_sizes, int n_in, void* d_out, int out_size, void* d_ws,
                              size_t ws_size, hipStream_t stream) {
  fused<<<256, 512, 0, stream>>>((const u16*)d_in[0], (const u16*)d_in[1], (const u16*)d_in[2], (const u16*)d_in[3],
                                 (const u16*)d_in[4], (const u16*)d_in[6], (const u16*)d_in[7], (const u16*)d_in[8],
                                 (const u16*)d_in[9], (const u16*)d_in[10], (char*)d_ws, (u16*)d_out);
}
```

```cpp
#define PROBE 0
#define PROBE_B 0
#include <hip/hip_runtime.h>
#include <hip/hip_bf16.h>

typedef unsigned short u16;
typedef unsigned long long u64;
typedef short bf16x8 __attribute__((ext_vector_type(8)));
typedef short bf16x4 __attribute__((ext_vector_type(4)));
typedef float f32x2 __attribute__((ext_vector_type(2)));
typedef float f32x4 __attribute__((ext_vector_type(4)));
typedef unsigned u32x2 __attribute__((ext_vector_type(2)));
typedef unsigned u32x4 __attribute__((ext_vector_type(4)));
#define LAS __attribute__((address_space(3)))
#define GAS __attribute__((address_space(1)))
#define RLX_AGENT __ATOMIC_RELAXED, __HIP_MEMORY_SCOPE_AGENT

#define WAITV(n) asm volatile("s_waitcnt vmcnt(%0)" ::"n"(n) : "memory")
#define WAITL0() asm volatile("s_waitcnt lgkmcnt(0)" ::: "memory")
#define BAR() do { asm volatile("" ::: "memory"); __builtin_amdgcn_s_barrier(); asm volatile("" ::: "memory"); } while (0)
#define SCHED() __builtin_amdgcn_sched_barrier(0)
#define SGB(mask, n) __builtin_amdgcn_sched_group_barrier(mask, n, 0)
#define MFMA(a, b, c) __builtin_amdgcn_mfma_f32_16x16x32_bf16(a, b, c, 0, 0, 0)
#define TRD(p) __builtin_amdgcn_ds_read_tr16_b64_v4i16((LAS bf16x4*)(p))
#define CAT(lo, hi) __builtin_shufflevector(lo, hi, 0, 1, 2, 3, 4, 5, 6, 7)

template <bool SC1 = false>
__device__ __forceinline__ void glds(const void* sbase, unsigned voff, unsigned l) {
  unsigned keep;
  l = __builtin_amdgcn_readfirstlane(l);
  if (SC1)
    asm volatile("s_mov_b32 %0, m0\n\ts_mov_b32 m0, %3\n\ts_nop 0\n\tglobal_load_lds_dwordx4 %1, %2 sc1\n\ts_mov_b32 m0, %0"
                 : "=&s"(keep) : "v"(voff), "s"(sbase), "s"(l) : "memory");
  else
    asm volatile("s_mov_b32 %0, m0\n\ts_mov_b32 m0, %3\n\ts_nop 0\n\tglobal_load_lds_dwordx4 %1, %2\n\ts_mov_b32 m0, %0"
                 : "=&s"(keep) : "v"(voff), "s"(sbase), "s"(l) : "memory");
}
__device__ __forceinline__ void touch(const void* sbase, unsigned voff, unsigned l) {
  unsigned keep;
  l = __builtin_amdgcn_readfirstlane(l);
  asm volatile("s_mov_b32 %0, m0\n\ts_mov_b32 m0, %3\n\ts_nop 0\n\tglobal_load_lds_dword %1, %2\n\ts_mov_b32 m0, %0"
               : "=&s"(keep) : "v"(voff), "s"(sbase), "s"(l) : "memory");
}

constexpr int S = 2048, D = 448;

__device__ __forceinline__ float bf2f(u16 v) { return __uint_as_float(((unsigned)v) << 16); }
__device__ __forceinline__ float bflo(unsigned v) { return __uint_as_float(v << 16); }
__device__ __forceinline__ float bfhi(unsigned v) { return __uint_as_float(v & 0xffff0000u); }
__device__ __forceinline__ unsigned pk_bf16(float lo, float hi) {
  typedef __bf16 bf2 __attribute__((ext_vector_type(2)));
  f32x2 v = {lo, hi};
  bf2 b = __builtin_convertvector(v, bf2);
  return *(unsigned*)&b;
}
__device__ __forceinline__ f32x2 gelu2(f32x2 v) {
  const float c0 = -2.0f * 0.7978845608028654f * 1.4426950408889634f;
  const float c1 = c0 * 0.044715f;
  const f32x2 u = v * ((v * v) * c1 + c0);
  f32x2 e = {__builtin_amdgcn_exp2f(u.x), __builtin_amdgcn_exp2f(u.y)};
  e = e + 1.0f;
  const f32x2 rc = {__builtin_amdgcn_rcpf(e.x), __builtin_amdgcn_rcpf(e.y)};
  return v * rc;
}
__device__ __forceinline__ f32x4 gelu4(f32x4 v) {
  const f32x2 lo = gelu2((f32x2){v[0], v[1]}), hi = gelu2((f32x2){v[2], v[3]});
  return (f32x4){lo.x, lo.y, hi.x, hi.y};
}
__device__ __forceinline__ float xsum(float s) {
  auto a = __builtin_amdgcn_permlane16_swap(__float_as_uint(s), __float_as_uint(s), false, false);
  s = __uint_as_float(a[0]) + __uint_as_float(a[1]);
  auto b = __builtin_amdgcn_permlane32_swap(__float_as_uint(s), __float_as_uint(s), false, false);
  return __uint_as_float(b[0]) + __uint_as_float(b[1]);
}

constexpr unsigned OFF_BT = 0;
constexpr unsigned OFF_O2 = OFF_BT + 1792 * S * 2;
constexpr unsigned OFF_G = OFF_O2 + 1792 * S * 2;
constexpr unsigned OFF_X = OFF_G + 8192;
constexpr unsigned OFF_F1 = OFF_X + 64 * 8 * 512;
constexpr unsigned OFF_F2 = OFF_F1 + 8192;
constexpr unsigned OFF_E = OFF_F2 + 8192;
constexpr unsigned WS_USED = OFF_E + 128;
constexpr u64 EPOCH_STEP = 0x9E3779B97F4A7C15ull;
constexpr unsigned SPIN_CAP = 1u << 20;

#define STAMP_DECL u64 ts0_ = 0, tsp_ = 0
#define STAMP(i) do { if ((i) == 0) { asm volatile("s_memrealtime %0\n\ts_waitcnt lgkmcnt(0)" : "=s"(ts0_) :: "memory"); tsp_ = ts0_; } \
    if ((i) == PROBE && (i) != 0) { SCHED(); asm volatile("s_memrealtime %0\n\ts_waitcnt lgkmcnt(0)" : "=s"(tsp_) :: "memory"); SCHED(); } } while (0)
#define STAMP_FLUSH() do { if (blockIdx.x == PROBE_B && wid == 0) { while (__builtin_amdgcn_s_memrealtime() < tsp_ + 6000) __builtin_amdgcn_s_sleep(1); } } while (0)

constexpr unsigned WR0 = 114688, WSL = 14336;
constexpr int STA = 16384, STB = 14336, STG = STA + STB, RING = 4, NT = 32, LEAD = 4;
constexpr unsigned SPARE = RING * STG, JUNK = SPARE + 32768;
__device__ __forceinline__ constexpr unsigned wo_loc(int c) { return c < 2 ? SPARE + c * 16384 : (c - 2) * 16384; }

__device__ __forceinline__ void wait_flags(GAS u64* p, u64 tag) {
  for (unsigned spins = 0; spins < SPIN_CAP; ++spins) {
    const u64 v = __hip_atomic_load(p, RLX_AGENT);
    if (__all(v == tag)) break;
    __builtin_amdgcn_s_sleep(1);
  }
  __builtin_amdgcn_fence(__ATOMIC_ACQUIRE, "wavefront");
}

struct Loader2 {
  const char* base; unsigned step, l0; int cls, nxt, slot;
  template <int P, bool SC1> __device__ __forceinline__ void issue(const unsigned (&off)[P]) {
    const int st = ((nxt & 3) == cls) ? ((nxt + LEAD) & (NT - 1)) : nxt;
    const char* g = base + (size_t)st * step;
    const unsigned l = l0 + slot * STG;
#pragma unroll
    for (int i = 0; i < P; ++i) glds<SC1>(g, off[i], l + i * 1024);
    ++nxt;
    slot = (slot + 1) & (RING - 1);
  }
};

__global__ __launch_bounds__(512, 2) void fused(const u16* __restrict__ x, const u16* __restrict__ wsr, const u16* __restrict__ wsi,
                                                const u16* __restrict__ wdr, const u16* __restrict__ wdi, const u16* __restrict__ win,
                                                const u16* __restrict__ wsp, const u16* __restrict__ bsp, const u16* __restrict__ wout,
                                                const u16* __restrict__ gw, char* __restrict__ ws, u16* __restrict__ out) {
  __shared__ __attribute__((aligned(1024))) char smem[163840];
  LAS char* lds = (LAS char*)smem;
  const int wid = __builtin_amdgcn_readfirstlane(threadIdx.x >> 6), lane = threadIdx.x & 63;
  STAMP_DECL; STAMP(0);
  const int b = blockIdx.x, xcd = b & 7, r = b >> 3;
  int mi, p, cls;
  if (xcd < 4) { mi = xcd * 4 + (r & 3); p = 8 + (r >> 2); cls = ((r >> 2) + r) & 3; }
  else { const int h = xcd - 4; mi = (h & 1) * 8 + (r & 7); p = (h >> 1) * 4 + (r >> 3); cls = ((r >> 3) + r) & 3; }
  const bool G = xcd < 4;
  const int N1 = G ? 14 : 7;
  const u64 ord = (u64)(cls == 0 ? 0x5463120u : cls == 1 ? 0x6532410u : cls == 2 ? 0x6542031u : 0x6423501u) * 0x10000001ull;
  const int rg = 4 * xcd + (r & 3), dg = r >> 2;
  u64 tag = *(const __attribute__((address_space(4))) u64*)(ws + OFF_E);
  const unsigned ldsb = __builtin_amdgcn_readfirstlane((unsigned)(size_t)lds);
  const unsigned l3 = lane >> 3, l7 = lane & 7, l4 = (lane >> 4) & 1;

  if (wid >= 4) {
    const int lw = wid - 4;
    const unsigned wo_off = l3 * 896 + (((((l7 >> 1) ^ ((lane >> 4) & 3)) << 1) | (l7 & 1)) << 4);
    const char* wo_base = (const char*)wout + (dg < 7 ? dg : 0) * 128 + (size_t)lw * 32 * 896;
    auto wo_chunk = [&](int c) {
      int cc = c + dg;
      if (cc >= 7) cc -= 7;
#pragma unroll
      for (int i = 0; i < 4; ++i) glds(wo_base + (size_t)(cc * 128 + i * 8) * 896, wo_off, ldsb + wo_loc(c) + lw * 4096 + i * 1024);
    };
    const unsigned warm_off = (unsigned)((r * 4 + lw) * 49 + (lane < 49 ? lane : 48)) * 128;
    if (lw < 2) {
      unsigned off[8];
      {
        const char* xg = (const char*)x + (size_t)(mi * 128 + lw * 64) * 896;
#pragma unroll
        for (int i = 0; i < 8; ++i) off[i] = (8 * i + l3) * 896 + ((l7 ^ (l4 | ((i & 3) << 1))) << 4);
        auto issue = [&](int s) {
          const unsigned st = (ord >> (4 * s)) & 7;
#pragma unroll
          for (int i = 0; i < 8; ++i) glds(xg + st * 128, off[i], ldsb + st * 16384 + lw * 8192 + i * 1024);
        };
        issue(0);
        issue(1);
        asm volatile("" : "+s"(tag));
        STAMP(1);
        WAITV(8);
        BAR();
        issue(2);
#pragma unroll 1
        for (int s = 0; s < 4; ++s) { WAITV(8); BAR(); issue(s + 3); }
        WAITV(8); BAR();
        WAITV(0);
#pragma unroll 1
        for (int s = 5; s < N1; ++s) BAR();
      }
      STAMP(2);
      const u16* am = p < 4 ? wsr : wsi;
      am = G ? wsp : am;
#pragma unroll
      for (int i = 0; i < 8; ++i) off[i] = (8 * i + l3) * 4096 + ((l7 ^ (l4 | ((i & 3) << 1))) << 4);
      Loader2 L{(const char*)am + (size_t)(mi * 128 + lw * 64) * 4096, 128, ldsb + lw * 8192, cls, 0, 0};
      L.issue<8, false>(off);
      L.issue<8, false>(off);
      if (!G) {
        const int row0 = mi * 128 + p * 16 + lw * 8;
        const int lc = lane < 56 ? lane : 55;
        const u32x4 gv = *(const u32x4*)(gw + lc * 8);
        u32x4 xv[8];
#pragma unroll
        for (int i = 0; i < 8; ++i) xv[i] = *(const u32x4*)(x + (size_t)(row0 + i) * D + lc * 8);
        float res = 0.f;
#pragma unroll
        for (int i = 0; i < 8; ++i) {
          float a = 0.f;
#pragma unroll
          for (int j = 0; j < 4; ++j) a += bflo(xv[i][j]) * bflo(gv[j]) + bfhi(xv[i][j]) * bfhi(gv[j]);
          a = lane < 56 ? a : 0.f;
#pragma unroll
          for (int o = 8; o >= 1; o >>= 1) a += __shfl_xor(a, o);
          a = xsum(a);
          if (lane == i) res = a;
        }
        if (lane < 8) __hip_atomic_store((GAS float*)(ws + OFF_G) + row0 + lane, 1.0f / (1.0f + __expf(-res)), RLX_AGENT);
      }
      L.issue<8, false>(off);
      L.issue<8, false>(off);
      WAITV(24);
      STAMP(3);
      BAR();
#pragma unroll 1
      for (int t = 0; t < NT - 4; ++t) {
        WAITV(16);
        BAR();
        if (L.nxt == 20 + cls) touch(wout, warm_off, ldsb + JUNK);
        L.issue<8, false>(off);
      }
      wo_chunk(0); wo_chunk(1);
      WAITV(24); BAR();
      wo_chunk(2);
      WAITV(20); BAR();
      wo_chunk(3); wo_chunk(4);
      WAITV(20); BAR();
    } else {
      const int l2 = lw - 2;
      unsigned off[7];
      {
        const char* wb; unsigned step;
        if (G) {
          wb = (const char*)win + (p - 8) * 224 + 1792; step = 64 * 3584;
#pragma unroll
          for (int i = 0; i < 7; ++i) {
            const unsigned u = (l2 * 7 + i) * 64 + lane, R = u / 14, c = u % 14;
            off[i] = (R ^ (((R >> 3) & 1) << 2)) * 3584 + c * 16;
          }
        } else {
          const u16* dm = p < 4 ? wdr : wdi;
          wb = (const char*)dm + (size_t)(p & 3) * 112 * 896; step = 128;
#pragma unroll
          for (int i = 0; i < 7; ++i) {
            const unsigned lr = 8 * (l2 * 7 + i) + l3;
            off[i] = lr * 896 + ((l7 ^ ((lr >> 1) & 7)) << 4);
          }
        }
        int slot = 0;
        auto issue = [&](int s) {
          const unsigned st = (ord >> (4 * s)) & 7;
          const char* g = wb + st * step - (s >= 7 ? 1792 : 0);
#pragma unroll
          for (int i = 0; i < 7; ++i) glds(g, off[i], ldsb + WR0 + slot * WSL + l2 * 7168 + i * 1024);
          slot = slot == 2 ? 0 : slot + 1;
        };
        issue(0);
        issue(1);
        asm volatile("" : "+s"(tag));
        STAMP(1);
        WAITV(7);
        BAR();
        issue(2);
#pragma unroll 1
        for (int s = 0; s < N1 - 3; ++s) { WAITV(7); BAR(); issue(s + 3); }
        WAITV(7); BAR();
        WAITV(0); BAR();
        BAR();
      }
      STAMP(2);
      tag += EPOCH_STEP;
#pragma unroll
      for (int i = 0; i < 7; ++i) off[i] = i * 1024 + lane * 16;
      Loader2 L{ws + OFF_BT + (size_t)p * 64 * 7 * 1024 + l2 * 7168, STB, ldsb + STA + l2 * 7168, cls, 0, 0};
      wait_flags((GAS u64*)(ws + OFF_F1) + p * 64 + lane, tag);
      STAMP(4);
      L.issue<7, true>(off);
      L.issue<7, true>(off);
      WAITV(7);
      STAMP(3);
      BAR();
      L.issue<7, true>(off);
      L.issue<7, true>(off);
#pragma unroll 1
      for (int t = 0; t < NT - 4; ++t) {
        WAITV(14);
        BAR();
        if (L.nxt == 20 + cls) touch(wout, warm_off, ldsb + JUNK);
        L.issue<7, true>(off);
      }
      wo_chunk(0); wo_chunk(1);
      WAITV(22); BAR();
      wo_chunk(2);
      WAITV(19); BAR();
      wo_chunk(3); wo_chunk(4);
      WAITV(20); BAR();
    }
    wo_chunk(5); wo_chunk(6);
    WAITV(0);
    STAMP(5);
    BAR();
    STAMP_FLUSH();
    return;
  }

  const int fi = lane & 15, fq = lane >> 4;
  const unsigned arow = 8 * (fi >> 2) + (fi & 3);
  const unsigned tq = fi >> 2, tp = lane & 3;
  const unsigned aoff0 = (32 * wid + arow) * 128 + ((fq ^ (fi >> 1)) << 4);
  const unsigned aoff1 = (32 * wid + arow) * 128 + (((fq + 4) ^ (fi >> 1)) << 4);
  const __amdgpu_buffer_rsrc_t rws = __builtin_amdgcn_make_buffer_rsrc(ws, 0, WS_USED, 0x00020000);
  float bb[2] = {bf2f(bsp[mi * 128 + 32 * wid + arow]), bf2f(bsp[mi * 128 + 32 * wid + arow + 4])};
  asm volatile("" : "+v"(bb[0]), "+v"(bb[1]), "+s"(tag));
  tag += EPOCH_STEP;
  f32x4 acc[2][7];
  auto zero_acc = [&]() {
#pragma unroll
    for (int m = 0; m < 2; ++m)
#pragma unroll
      for (int n = 0; n < 7; ++n) { acc[m][n] = (f32x4){0, 0, 0, 0}; asm volatile("" : "+v"(acc[m][n])); }
  };
  zero_acc();
  u32x2 z1p[2][7];
  const unsigned bt_off = OFF_BT + ((p * 64 + 4 * mi + wid) * 7) * 1024;
  GAS u64* f1 = (GAS u64*)(ws + OFF_F1) + p * 64 + 4 * mi + wid;
  if (G) {
    bf16x8 fa[2][2];
    bf16x4 fl[2][7], fh[2][7];
    const unsigned b0 = WR0 + (8 * fq + 4 * (fq & 1) + tq) * 224 + tp * 8;
    const unsigned b1 = WR0 + (8 * fq + 4 * (1 - (fq & 1)) + tq) * 224 + tp * 8;
    int slot = 0;
    auto rd = [&](int k, int s, int sl) {
      LAS char* lx = lds + ((ord >> (4 * s)) & 7) * 16384 + (k ? aoff1 : aoff0);
      LAS char* lw = lds + sl * WSL + k * 7168;
#pragma unroll
      for (int h = 0; h < 2; ++h) fa[k][h] = *(const LAS bf16x8*)(lx + h * 512);
#pragma unroll
      for (int n = 0; n < 7; ++n) { fl[k][n] = TRD(lw + b0 + n * 32); fh[k][n] = TRD(lw + b1 + n * 32); }
    };
    auto mm = [&](int k) {
#pragma unroll
      for (int n = 0; n < 7; ++n)
#pragma unroll
        for (int h = 0; h < 2; ++h) acc[h][n] = MFMA(CAT(fl[k][n], fh[k][n]), fa[k][h], acc[h][n]);
      SGB(0x8, 1); SGB(0x100, 2); SGB(0x8, 1); SGB(0x100, 2);
#pragma unroll
      for (int i = 0; i < 12; ++i) { SGB(0x8, 1); SGB(0x100, 1); }
      SCHED();
    };
    auto run = [&](int s0, int s1) {
#pragma unroll 1
      for (int s = s0; s < s1; ++s) {
        rd(1, s, slot); mm(0);
        slot = slot == 2 ? 0 : slot + 1;
        WAITL0(); BAR();
        rd(0, s + 1, slot); mm(1);
      }
    };
    BAR();
    STAMP(1);
    rd(0, 0, 0);
    SCHED();
    run(0, 7);
    STAMP(2);
    f32x4 z2[2][7];
    GAS u64* xch = (GAS u64*)(ws + OFF_X) + (mi * 4 + wid) * 8 * 64 + lane;
    const u64 tag40 = tag << 24;
    {
      float mean_p[2], m2_p[2];
#pragma unroll
      for (int h = 0; h < 2; ++h) {
        f32x4 s4 = {0, 0, 0, 0};
#pragma unroll
        for (int n = 0; n < 7; ++n) { z2[h][n] = gelu4(acc[h][n]); s4 += z2[h][n]; }
        mean_p[h] = xsum((s4[0] + s4[1]) + (s4[2] + s4[3])) * (1.0f / 112.0f);
      }
#pragma unroll
      for (int h = 0; h < 2; ++h) {
        f32x4 q4 = {0, 0, 0, 0};
#pragma unroll
        for (int n = 0; n < 7; ++n) { const f32x4 d = z2[h][n] - mean_p[h]; q4 += d * d; }
        m2_p[h] = xsum((q4[0] + q4[1]) + (q4[2] + q4[3]));
      }
      const float val = (fq & 2) ? ((fq & 1) ? m2_p[1] : m2_p[0]) : ((fq & 1) ? mean_p[1] : mean_p[0]);
      const unsigned bits = (__float_as_uint(val) + 0x80u) >> 8;
      __hip_atomic_store(xch + (p - 8) * 64, tag40 | bits, RLX_AGENT);
    }
    zero_acc();
    STAMP(3);
    run(7, 10);
    u64 gr[8];
#pragma unroll
    for (int c = 0; c < 8; ++c) gr[c] = __hip_atomic_load(xch + c * 64, RLX_AGENT);
    SCHED();
    run(10, 11);
    STAMP(4);
    for (unsigned spins = 0;; ++spins) {
      unsigned bh = 0, bl = 0;
#pragma unroll
      for (int c = 0; c < 8; ++c) {
        bh |= (unsigned)(gr[c] >> 32) ^ (unsigned)(tag40 >> 32);
        bl |= (unsigned)gr[c] ^ (unsigned)tag40;
      }
      if (__all((bh | (bl >> 24)) == 0) || spins > SPIN_CAP) break;
#pragma unroll
      for (int c = 0; c < 8; ++c) gr[c] = __hip_atomic_load(xch + c * 64, RLX_AGENT);
    }
    STAMP(5);
    {
      float pv[8], sum = 0.f, dev = 0.f;
#pragma unroll
      for (int c = 0; c < 8; ++c) { pv[c] = __uint_as_float((unsigned)gr[c] << 8); sum += pv[c]; }
      const float mean = sum * 0.125f;
#pragma unroll
      for (int c = 0; c < 8; ++c) { const float d = pv[c] - mean; dev = __builtin_fmaf(d, d, dev); }
      const float mine = (fq & 2) ? sum : dev * 112.0f;
      auto sm = __builtin_amdgcn_permlane32_swap(__float_as_uint(mine), __float_as_uint(mine), false, false);
      const float rstd = __builtin_amdgcn_rsqf((__uint_as_float(sm[0]) + __uint_as_float(sm[1])) * (1.0f / 896.0f) + 1e-5f);
      auto ml = __builtin_amdgcn_permlane32_swap(__float_as_uint(mean), __float_as_uint(mean), false, false);
      const float nm = -__uint_as_float(ml[0]) * rstd;
      auto rs = __builtin_amdgcn_permlane16_swap(__float_as_uint(rstd), __float_as_uint(rstd), false, false);
      auto mu = __builtin_amdgcn_permlane16_swap(__float_as_uint(nm), __float_as_uint(nm), false, false);
      LAS char* sc = lds + ((ord >> (4 * (wid >> 1))) & 7) * 16384 + (wid & 1) * 8192;
      const unsigned wsw = ((fi & 7) << 2) ^ (fi >> 2);
#pragma unroll
      for (int h = 0; h < 2; ++h)
#pragma unroll
        for (int n = 0; n < 7; ++n) {
          const f32x4 v = z2[h][n] * __uint_as_float(rs[h]) + __uint_as_float(mu[h]);
          const u32x2 o = {pk_bf16(v[0], v[1]), pk_bf16(v[2], v[3])};
          *(LAS u32x2*)(sc + (arow + 4 * h) * 256 + (((4 * n + fq) ^ wsw) << 3)) = o;
        }
      WAITL0();
      const unsigned ri = (fq << 2) | tq, rsw = ((ri & 7) << 2) ^ (ri >> 2);
#pragma unroll
      for (int n = 0; n < 7; ++n) {
        const unsigned a = (8 * fq + tq) * 256 + (((4 * n + tp) ^ rsw) << 3);
        const bf16x8 o = CAT(TRD(sc + a), TRD(sc + a + 1024));
        __builtin_amdgcn_raw_buffer_store_b128(*(const u32x4*)&o, rws, lane * 16, bt_off + n * 1024, 16);
      }
    }
    SCHED();
    STAMP(6);
    run(11, 13);
    WAITV(0);
    if (lane == 0) __hip_atomic_store(f1, tag, RLX_AGENT);
    STAMP(7);
    run(13, 14);
    STAMP(8);
#pragma unroll
    for (int h = 0; h < 2; ++h)
#pragma unroll
      for (int n = 0; n < 7; ++n) {
        const f32x4 v = gelu4(acc[h][n]);
        z1p[h][n] = (u32x2){pk_bf16(v[0], v[1]), pk_bf16(v[2], v[3])};
      }
  } else {
    bf16x8 fa[2][2], fb[2][7];
    const unsigned b0 = WR0 + fi * 128 + ((fq ^ (fi >> 1)) << 4);
    const unsigned b1 = WR0 + fi * 128 + (((fq + 4) ^ (fi >> 1)) << 4);
    int slot = 0;
    auto rd = [&](int k, int s, int sl) {
      LAS char* lx = lds + ((ord >> (4 * s)) & 7) * 16384 + (k ? aoff1 : aoff0);
      LAS char* lw = lds + sl * WSL + (k ? b1 : b0);
#pragma unroll
      for (int h = 0; h < 2; ++h) fa[k][h] = *(const LAS bf16x8*)(lx + h * 512);
#pragma unroll
      for (int n = 0; n < 7; ++n) fb[k][n] = *(const LAS bf16x8*)(lw + n * 2048);
    };
    auto mm = [&](int k) {
#pragma unroll
      for (int n = 0; n < 7; ++n)
#pragma unroll
        for (int h = 0; h < 2; ++h) acc[h][n] = MFMA(fa[k][h], fb[k][n], acc[h][n]);
#pragma unroll
      for (int i = 0; i < 9; ++i) { SGB(0x8, 1); SGB(0x100, 1); }
      SGB(0x8, 5);
      SCHED();
    };
    BAR();
    STAMP(1);
    rd(0, 0, 0);
    SCHED();
#pragma unroll 1
    for (int s = 0; s < 7; ++s) {
      rd(1, s, slot); mm(0);
      slot = slot == 2 ? 0 : slot + 1;
      WAITL0(); BAR();
      rd(0, s + 1, slot); mm(1);
    }
    STAMP(2);
#pragma unroll
    for (int n = 0; n < 7; ++n) {
      const u32x4 o = {pk_bf16(acc[0][n][0], acc[0][n][1]), pk_bf16(acc[0][n][2], acc[0][n][3]),
                       pk_bf16(acc[1][n][0], acc[1][n][1]), pk_bf16(acc[1][n][2], acc[1][n][3])};
      __builtin_amdgcn_raw_buffer_store_b128(o, rws, lane * 16, bt_off + n * 1024, 16);
    }
    STAMP(6);
    WAITV(0);
    if (lane == 0) __hip_atomic_store(f1, tag, RLX_AGENT);
    STAMP(7);
#pragma unroll
    for (int h = 0; h < 2; ++h)
#pragma unroll
      for (int n = 0; n < 7; ++n) z1p[h][n] = (u32x2){0x3f803f80u, 0x3f803f80u};
    bb[0] = bb[1] = 0.f;
  }
  zero_acc();
  {
    const unsigned boff = STA + lane * 16;
    bf16x8 fa[2][2][2], fb[2][2][7];
    auto rd = [&](int set, int slot) {
      LAS char* l = lds + slot * STG;
#pragma unroll
      for (int k = 0; k < 2; ++k) {
#pragma unroll
        for (int m = 0; m < 2; ++m) fa[set][k][m] = *(const LAS bf16x8*)(l + (k ? aoff1 : aoff0) + m * 512);
#pragma unroll
        for (int n = 0; n < 7; ++n) fb[set][k][n] = *(const LAS bf16x8*)(l + boff + k * 7168 + n * 1024);
      }
    };
    auto mm = [&](int set) {
#pragma unroll
      for (int k = 0; k < 2; ++k)
#pragma unroll
        for (int n = 0; n < 7; ++n)
#pragma unroll
          for (int m = 0; m < 2; ++m) acc[m][n] = MFMA(fb[set][k][n], fa[set][k][m], acc[m][n]);
    };
    auto sched = [&]() {
#pragma unroll
      for (int i = 0; i < 18; ++i) { SGB(0x8, 1); SGB(0x100, 1); }
      SGB(0x8, 10);
      SCHED();
    };
    STAMP(9);
    WAITL0();
    BAR();
    STAMP(10);
    rd(0, 0);
    int slot = 1;
#pragma unroll 1
    for (int t = 0; t < NT - 2; t += 2) {
      WAITL0(); BAR();
      rd(1, slot); mm(0); sched();
      slot = (slot + 1) & (RING - 1);
      WAITL0(); BAR();
      rd(0, slot); mm(1); sched();
      slot = (slot + 1) & (RING - 1);
    }
    WAITL0(); BAR();
    rd(1, slot); mm(0); sched();
    mm(1);
    SCHED();
  }
  STAMP(11);
  {
    const int odd = p & 1;
    u32x2 o[2][7];
#pragma unroll
    for (int m = 0; m < 2; ++m)
#pragma unroll
      for (int n = 0; n < 7; ++n) {
        const f32x4 z = {bflo(z1p[m][n][0]), bfhi(z1p[m][n][0]), bflo(z1p[m][n][1]), bfhi(z1p[m][n][1])};
        const f32x4 v = (acc[m][n] + bb[m]) * z;
        o[m][n] = (u32x2){pk_bf16(v[0], v[1]), pk_bf16(v[2], v[3])};
      }
    const unsigned o2 = OFF_O2 + ((mi * 8 + wid * 2) * 56 + ((7 * p) >> 1)) * 1024;
#pragma unroll
    for (int m = 0; m < 2; ++m) {
      const unsigned om = o2 + m * 56 * 1024;
      if (odd) {
        __builtin_amdgcn_raw_buffer_store_b64(o[m][0], rws, lane * 16 + 8, om, 16);
#pragma unroll
        for (int k = 0; k < 3; ++k)
          __builtin_amdgcn_raw_buffer_store_b128((u32x4){o[m][2 * k + 1][0], o[m][2 * k + 1][1], o[m][2 * k + 2][0], o[m][2 * k + 2][1]},
                                                 rws, lane * 16, om + (k + 1) * 1024, 16);
      } else {
#pragma unroll
        for (int k = 0; k < 3; ++k)
          __builtin_amdgcn_raw_buffer_store_b128((u32x4){o[m][2 * k][0], o[m][2 * k][1], o[m][2 * k + 1][0], o[m][2 * k + 1][1]},
                                                 rws, lane * 16, om + k * 1024, 16);
        __builtin_amdgcn_raw_buffer_store_b64(o[m][6], rws, lane * 16, om + 3 * 1024, 16);
      }
    }
  }
  STAMP(12);
  WAITV(0);
  if (lane == 0) __hip_atomic_store((GAS u64*)(ws + OFF_F2) + (4 * mi + wid) * 16 + p, tag, RLX_AGENT);
  STAMP(13);
  BAR();
  if (r >= 28) { STAMP_FLUSH(); return; }
  const int mt = 4 * rg + wid;
  unsigned wb[4];
#pragma unroll
  for (int n = 0; n < 4; ++n) wb[n] = (4 * fq + tq) * 128 + ((n ^ (((4 * fq + tq) >> 1) & 3)) << 5) + tp * 8;
  bf16x4 wl[2][16], wh[2][16];
  auto rdw = [&](int c) {
#pragma unroll
    for (int i = 0; i < 16; ++i) {
      LAS char* a = lds + wb[i & 3] + (wo_loc(c) + (i >> 2) * 4096);
      wl[c & 1][i] = TRD(a);
      wh[c & 1][i] = TRD(a + 2048);
    }
  };
  rdw(0);
  SCHED();
  wait_flags((GAS u64*)(ws + OFF_F2) + (mt >> 1) * 16 + fi, tag);
  STAMP(14);
  if (b == 0 && threadIdx.x == 0) __hip_atomic_store((GAS u64*)(ws + OFF_E), tag, RLX_AGENT);
  const unsigned o2 = OFF_O2 + mt * 56 * 1024;
  u32x4 hf[7][4];
  auto ldh = [&](int c) {
    int cc = c + dg;
    if (cc >= 7) cc -= 7;
#pragma unroll
    for (int k = 0; k < 4; ++k) hf[c][k] = __builtin_amdgcn_raw_buffer_load_b128(rws, lane * 16 + k * 1024, o2 + (28 + cc * 4) * 1024, 16);
  };
  ldh(0); ldh(1); ldh(2); ldh(3);
  SCHED();
  u32x4 yr[2], yi[2];
  float g;
  const int t = 32 * (mt >> 1) + arow + 4 * (mt & 1);
  f32x4 oc[4];
#pragma unroll
  for (int n = 0; n < 4; ++n) oc[n] = (f32x4){0, 0, 0, 0};
#pragma unroll
  for (int c = 0; c < 7; ++c) {
    if (c < 3) ldh(c + 4);
    if (c == 3) {
#pragma unroll
      for (int k = 0; k < 2; ++k) {
        yr[k] = __builtin_amdgcn_raw_buffer_load_b128(rws, lane * 16 + k * 1024, o2 + 2 * dg * 1024, 16);
        yi[k] = __builtin_amdgcn_raw_buffer_load_b128(rws, lane * 16 + k * 1024, o2 + (14 + 2 * dg) * 1024, 16);
      }
      g = __hip_atomic_load((GAS float*)(ws + OFF_G) + t, RLX_AGENT);
    }
    SCHED();
    if (c < 6) rdw(c + 1);
#pragma unroll
    for (int i = 0; i < 16; ++i) oc[i & 3] = MFMA(CAT(wl[c & 1][i], wh[c & 1][i]), *(const bf16x8*)&hf[c][i >> 2], oc[i & 3]);
    if (c < 6) {
#pragma unroll
      for (int i = 0; i < 16; ++i) { SGB(0x8, 1); SGB(0x100, 2); }
    }
    SCHED();
  }
  STAMP(15);
  const float g1 = 1.0f - g;
#pragma unroll
  for (int n = 0; n < 4; ++n) {
    const unsigned r0 = yr[n >> 1][(n & 1) * 2], r1 = yr[n >> 1][(n & 1) * 2 + 1];
    const unsigned i0 = yi[n >> 1][(n & 1) * 2], i1 = yi[n >> 1][(n & 1) * 2 + 1];
    const f32x4 y = {bflo(r0) - bflo(i0), bfhi(r0) - bfhi(i0), bflo(r1) - bflo(i1), bfhi(r1) - bfhi(i1)};
    const f32x4 v = y * g + oc[n] * g1;
    *(u32x2*)(out + (size_t)t * D + 64 * dg + 16 * n + 4 * fq) = (u32x2){pk_bf16(v[0], v[1]), pk_bf16(v[2], v[3])};
  }
  STAMP(16);
  STAMP_FLUSH();
}

extern "C" void kernel_launch(void* const* d_in, const int* in_sizes, int n_in, void* d_out, int out_size, void* d_ws,
                              size_t ws_size, hipStream_t stream) {
  fused<<<256, 512, 0, stream>>>((const u16*)d_in[0], (const u16*)d_in[1], (const u16*)d_in[2], (const u16*)d_in[3],
                                 (const u16*)d_in[4], (const u16*)d_in[6], (const u16*)d_in[7], (const u16*)d_in[8],
                                 (const u16*)d_in[9], (const u16*)d_in[10], (char*)d_ws, (u16*)d_out);
}
```

```cpp
#include <hip/hip_runtime.h>
#include <hip/hip_bf16.h>

typedef unsigned short u16;
typedef unsigned long long u64;
typedef short bf16x8 __attribute__((ext_vector_type(8)));
typedef short bf16x4 __attribute__((ext_vector_type(4)));
typedef float f32x2 __attribute__((ext_vector_type(2)));
typedef float f32x4 __attribute__((ext_vector_type(4)));
typedef unsigned u32x2 __attribute__((ext_vector_type(2)));
typedef unsigned u32x4 __attribute__((ext_vector_type(4)));
#define LAS __attribute__((address_space(3)))
#define GAS __attribute__((address_space(1)))
#define RLX_AGENT __ATOMIC_RELAXED, __HIP_MEMORY_SCOPE_AGENT

#define WAITV(n) asm volatile("s_waitcnt vmcnt(%0)" ::"n"(n) : "memory")
#define WAITL0() asm volatile("s_waitcnt lgkmcnt(0)" ::: "memory")
#define BAR() do { asm volatile("" ::: "memory"); __builtin_amdgcn_s_barrier(); asm volatile("" ::: "memory"); } while (0)
#define SCHED() __builtin_amdgcn_sched_barrier(0)
#define SGB(mask, n) __builtin_amdgcn_sched_group_barrier(mask, n, 0)
#define MFMA(a, b, c) __builtin_amdgcn_mfma_f32_16x16x32_bf16(a, b, c, 0, 0, 0)
#define TRD(p) __builtin_amdgcn_ds_read_tr16_b64_v4i16((LAS bf16x4*)(p))
#define CAT(lo, hi) __builtin_shufflevector(lo, hi, 0, 1, 2, 3, 4, 5, 6, 7)

template <bool SC1 = false>
__device__ __forceinline__ void glds(const void* sbase, unsigned voff, unsigned l) {
  unsigned keep;
  l = __builtin_amdgcn_readfirstlane(l);
  if (SC1)
    asm volatile("s_mov_b32 %0, m0\n\ts_mov_b32 m0, %3\n\ts_nop 0\n\tglobal_load_lds_dwordx4 %1, %2 sc1\n\ts_mov_b32 m0, %0"
                 : "=&s"(keep) : "v"(voff), "s"(sbase), "s"(l) : "memory");
  else
    asm volatile("s_mov_b32 %0, m0\n\ts_mov_b32 m0, %3\n\ts_nop 0\n\tglobal_load_lds_dwordx4 %1, %2\n\ts_mov_b32 m0, %0"
                 : "=&s"(keep) : "v"(voff), "s"(sbase), "s"(l) : "memory");
}
__device__ __forceinline__ void touch(const void* sbase, unsigned voff, unsigned l) {
  unsigned keep;
  l = __builtin_amdgcn_readfirstlane(l);
  asm volatile("s_mov_b32 %0, m0\n\ts_mov_b32 m0, %3\n\ts_nop 0\n\tglobal_load_lds_dword %1, %2\n\ts_mov_b32 m0, %0"
               : "=&s"(keep) : "v"(voff), "s"(sbase), "s"(l) : "memory");
}

constexpr int S = 2048, D = 448;

__device__ __forceinline__ float bf2f(u16 v) { return __uint_as_float(((unsigned)v) << 16); }
__device__ __forceinline__ float bflo(unsigned v) { return __uint_as_float(v << 16); }
__device__ __forceinline__ float bfhi(unsigned v) { return __uint_as_float(v & 0xffff0000u); }
__device__ __forceinline__ unsigned pk_bf16(float lo, float hi) {
  typedef __bf16 bf2 __attribute__((ext_vector_type(2)));
  f32x2 v = {lo, hi};
  bf2 b = __builtin_convertvector(v, bf2);
  return *(unsigned*)&b;
}
__device__ __forceinline__ f32x2 gelu2(f32x2 v) {
  const float c0 = -2.0f * 0.7978845608028654f * 1.4426950408889634f;
  const float c1 = c0 * 0.044715f;
  const f32x2 u = v * ((v * v) * c1 + c0);
  f32x2 e = {__builtin_amdgcn_exp2f(u.x), __builtin_amdgcn_exp2f(u.y)};
  e = e + 1.0f;
  const f32x2 rc = {__builtin_amdgcn_rcpf(e.x), __builtin_amdgcn_rcpf(e.y)};
  return v * rc;
}
__device__ __forceinline__ f32x4 gelu4(f32x4 v) {
  const f32x2 lo = gelu2((f32x2){v[0], v[1]}), hi = gelu2((f32x2){v[2], v[3]});
  return (f32x4){lo.x, lo.y, hi.x, hi.y};
}
__device__ __forceinline__ float xsum(float s) {
  auto a = __builtin_amdgcn_permlane16_swap(__float_as_uint(s), __float_as_uint(s), false, false);
  s = __uint_as_float(a[0]) + __uint_as_float(a[1]);
  auto b = __builtin_amdgcn_permlane32_swap(__float_as_uint(s), __float_as_uint(s), false, false);
  return __uint_as_float(b[0]) + __uint_as_float(b[1]);
}

constexpr unsigned OFF_BT = 0;
constexpr unsigned OFF_O2 = OFF_BT + 1792 * S * 2;
constexpr unsigned OFF_G = OFF_O2 + 1792 * S * 2;
constexpr unsigned OFF_X = OFF_G + 8192;
constexpr unsigned OFF_F1 = OFF_X + 64 * 8 * 512;
constexpr unsigned OFF_F2 = OFF_F1 + 8192;
constexpr unsigned OFF_E = OFF_F2 + 8192;
constexpr unsigned WS_USED = OFF_E + 128;
constexpr u64 EPOCH_STEP = 0x9E3779B97F4A7C15ull;
constexpr unsigned SPIN_CAP = 1u << 20;

#define STAMP_DECL
#define STAMP(i)
#define STAMP_FLUSH()

constexpr unsigned WR0 = 114688, WSL = 14336;
constexpr int STA = 16384, STB = 14336, STG = STA + STB, RING = 4, NT = 32, LEAD = 4;
constexpr unsigned SPARE = RING * STG, JUNK = SPARE + 32768;
__device__ __forceinline__ constexpr unsigned wo_loc(int c) { return c < 2 ? SPARE + c * 16384 : (c - 2) * 16384; }

__device__ __forceinline__ void wait_flags(GAS u64* p, u64 tag) {
  for (unsigned spins = 0; spins < SPIN_CAP; ++spins) {
    const u64 v = __hip_atomic_load(p, RLX_AGENT);
    if (__all(v == tag)) break;
    __builtin_amdgcn_s_sleep(1);
  }
  __builtin_amdgcn_fence(__ATOMIC_ACQUIRE, "wavefront");
}

struct Loader2 {
  const char* base; unsigned step, l0; int cls, nxt, slot;
  template <int P, bool SC1> __device__ __forceinline__ void issue(const unsigned (&off)[P]) {
    const int st = ((nxt & 3) == cls) ? ((nxt + LEAD) & (NT - 1)) : nxt;
    const char* g = base + (size_t)st * step;
    const unsigned l = l0 + slot * STG;
#pragma unroll
    for (int i = 0; i < P; ++i) glds<SC1>(g, off[i], l + i * 1024);
    ++nxt;
    slot = (slot + 1) & (RING - 1);
  }
};

__global__ __launch_bounds__(512, 2) void fused(const u16* __restrict__ x, const u16* __restrict__ wsr, const u16* __restrict__ wsi,
                                                const u16* __restrict__ wdr, const u16* __restrict__ wdi, const u16* __restrict__ win,
                                                const u16* __restrict__ wsp, const u16* __restrict__ bsp, const u16* __restrict__ wout,
                                                const u16* __restrict__ gw, char* __restrict__ ws, u16* __restrict__ out) {
  __shared__ __attribute__((aligned(1024))) char smem[163840];
  LAS char* lds = (LAS char*)smem;
  const int wid = __builtin_amdgcn_readfirstlane(threadIdx.x >> 6), lane = threadIdx.x & 63;
  STAMP_DECL; STAMP(0);
  const int b = blockIdx.x, xcd = b & 7, r = b >> 3;
  int mi, p, cls;
  if (xcd < 4) { mi = xcd * 4 + (r & 3); p = 8 + (r >> 2); cls = ((r >> 2) + r) & 3; }
  else { const int h = xcd - 4; mi = (h & 1) * 8 + (r & 7); p = (h >> 1) * 4 + (r >> 3); cls = ((r >> 3) + r) & 3; }
  const bool G = xcd < 4;
  const int N1 = G ? 14 : 7;
  const u64 ord = (u64)(cls == 0 ? 0x5463120u : cls == 1 ? 0x6532410u : cls == 2 ? 0x6542031u : 0x6423501u) * 0x10000001ull;
  const int rg = 4 * xcd + (r & 3), dg = r >> 2;
  u64 tag = *(const __attribute__((address_space(4))) u64*)(ws + OFF_E);
  const unsigned ldsb = __builtin_amdgcn_readfirstlane((unsigned)(size_t)lds);
  const unsigned l3 = lane >> 3, l7 = lane & 7, l4 = (lane >> 4) & 1;

  if (wid >= 4) {
    const int lw = wid - 4;
    const unsigned wo_off = l3 * 896 + (((((l7 >> 1) ^ ((lane >> 4) & 3)) << 1) | (l7 & 1)) << 4);
    const char* wo_base = (const char*)wout + (dg < 7 ? dg : 0) * 128 + (size_t)lw * 32 * 896;
    auto wo_chunk = [&](int c) {
      int cc = c + dg;
      if (cc >= 7) cc -= 7;
#pragma unroll
      for (int i = 0; i < 4; ++i) glds(wo_base + (size_t)(cc * 128 + i * 8) * 896, wo_off, ldsb + wo_loc(c) + lw * 4096 + i * 1024);
    };
    const unsigned warm_off = (unsigned)((r * 4 + lw) * 49 + (lane < 49 ? lane : 48)) * 128;
    if (lw < 2) {
      unsigned off[8];
      {
        const char* xg = (const char*)x + (size_t)(mi * 128 + lw * 64) * 896;
#pragma unroll
        for (int i = 0; i < 8; ++i) off[i] = (8 * i + l3) * 896 + ((l7 ^ (l4 | ((i & 3) << 1))) << 4);
        auto issue = [&](int s) {
          const unsigned st = (ord >> (4 * s)) & 7;
#pragma unroll
          for (int i = 0; i < 8; ++i) glds(xg + st * 128, off[i], ldsb + st * 16384 + lw * 8192 + i * 1024);
        };
        issue(0);
        issue(1);
        asm volatile("" : "+s"(tag));
        STAMP(1);
        WAITV(8);
        BAR();
        issue(2);
#pragma unroll 1
        for (int s = 0; s < 4; ++s) { WAITV(8); BAR(); issue(s + 3); }
        WAITV(8); BAR();
        WAITV(0);
#pragma unroll 1
        for (int s = 5; s < N1; ++s) BAR();
      }
      STAMP(2);
      const u16* am = p < 4 ? wsr : wsi;
      am = G ? wsp : am;
#pragma unroll
      for (int i = 0; i < 8; ++i) off[i] = (8 * i + l3) * 4096 + ((l7 ^ (l4 | ((i & 3) << 1))) << 4);
      Loader2 L{(const char*)am + (size_t)(mi * 128 + lw * 64) * 4096, 128, ldsb + lw * 8192, cls, 0, 0};
      L.issue<8, false>(off);
      L.issue<8, false>(off);
      if (!G) {
        const int row0 = mi * 128 + p * 16 + lw * 8;
        const int lc = lane < 56 ? lane : 55;
        const u32x4 gv = *(const u32x4*)(gw + lc * 8);
        u32x4 xv[8];
#pragma unroll
        for (int i = 0; i < 8; ++i) xv[i] = *(const u32x4*)(x + (size_t)(row0 + i) * D + lc * 8);
        float res = 0.f;
#pragma unroll
        for (int i = 0; i < 8; ++i) {
          float a = 0.f;
#pragma unroll
          for (int j = 0; j < 4; ++j) a += bflo(xv[i][j]) * bflo(gv[j]) + bfhi(xv[i][j]) * bfhi(gv[j]);
          a = lane < 56 ? a : 0.f;
#pragma unroll
          for (int o = 8; o >= 1; o >>= 1) a += __shfl_xor(a, o);
          a = xsum(a);
          if (lane == i) res = a;
        }
        if (lane < 8) __hip_atomic_store((GAS float*)(ws + OFF_G) + row0 + lane, 1.0f / (1.0f + __expf(-res)), RLX_AGENT);
      }
      L.issue<8, false>(off);
      L.issue<8, false>(off);
      WAITV(24);
      STAMP(3);
      BAR();
#pragma unroll 1
      for (int t = 0; t < NT - 4; ++t) {
        WAITV(16);
        BAR();
        if (L.nxt == 20 + cls) touch(wout, warm_off, ldsb + JUNK);
        L.issue<8, false>(off);
      }
      wo_chunk(0); wo_chunk(1);
      WAITV(24); BAR();
      wo_chunk(2);
      WAITV(20); BAR();
      wo_chunk(3); wo_chunk(4);
      WAITV(20); BAR();
    } else {
      const int l2 = lw - 2;
      unsigned off[7];
      {
        const char* wb; unsigned step;
        if (G) {
          wb = (const char*)win + (p - 8) * 224 + 1792; step = 64 * 3584;
#pragma unroll
          for (int i = 0; i < 7; ++i) {
            const unsigned u = (l2 * 7 + i) * 64 + lane, R = u / 14, c = u % 14;
            off[i] = (R ^ (((R >> 3) & 1) << 2)) * 3584 + c * 16;
          }
        } else {
          const u16* dm = p < 4 ? wdr : wdi;
          wb = (const char*)dm + (size_t)(p & 3) * 112 * 896; step = 128;
#pragma unroll
          for (int i = 0; i < 7; ++i) {
            const unsigned lr = 8 * (l2 * 7 + i) + l3;
            off[i] = lr * 896 + ((l7 ^ ((lr >> 1) & 7)) << 4);
          }
        }
        int slot = 0;
        auto issue = [&](int s) {
          const unsigned st = (ord >> (4 * s)) & 7;
          const char* g = wb + st * step - (s >= 7 ? 1792 : 0);
#pragma unroll
          for (int i = 0; i < 7; ++i) glds(g, off[i], ldsb + WR0 + slot * WSL + l2 * 7168 + i * 1024);
          slot = slot == 2 ? 0 : slot + 1;
        };
        issue(0);
        issue(1);
        asm volatile("" : "+s"(tag));
        STAMP(1);
        WAITV(7);
        BAR();
        issue(2);
#pragma unroll 1
        for (int s = 0; s < N1 - 3; ++s) { WAITV(7); BAR(); issue(s + 3); }
        WAITV(7); BAR();
        WAITV(0); BAR();
        BAR();
      }
      STAMP(2);
      tag += EPOCH_STEP;
#pragma unroll
      for (int i = 0; i < 7; ++i) off[i] = i * 1024 + lane * 16;
      Loader2 L{ws + OFF_BT + (size_t)p * 64 * 7 * 1024 + l2 * 7168, STB, ldsb + STA + l2 * 7168, cls, 0, 0};
      wait_flags((GAS u64*)(ws + OFF_F1) + p * 64 + lane, tag);
      STAMP(4);
      L.issue<7, true>(off);
      L.issue<7, true>(off);
      WAITV(7);
      STAMP(3);
      BAR();
      L.issue<7, true>(off);
      L.issue<7, true>(off);
#pragma unroll 1
      for (int t = 0; t < NT - 4; ++t) {
        WAITV(14);
        BAR();
        if (L.nxt == 20 + cls) touch(wout, warm_off, ldsb + JUNK);
        L.issue<7, true>(off);
      }
      wo_chunk(0); wo_chunk(1);
      WAITV(22); BAR();
      wo_chunk(2);
      WAITV(19); BAR();
      wo_chunk(3); wo_chunk(4);
      WAITV(20); BAR();
    }
    wo_chunk(5); wo_chunk(6);
    WAITV(0);
    STAMP(5);
    BAR();
    STAMP_FLUSH();
    return;
  }

  const int fi = lane & 15, fq = lane >> 4;
  const unsigned arow = 8 * (fi >> 2) + (fi & 3);
  const unsigned tq = fi >> 2, tp = lane & 3;
  const unsigned aoff0 = (32 * wid + arow) * 128 + ((fq ^ (fi >> 1)) << 4);
  const unsigned aoff1 = (32 * wid + arow) * 128 + (((fq + 4) ^ (fi >> 1)) << 4);
  const __amdgpu_buffer_rsrc_t rws = __builtin_amdgcn_make_buffer_rsrc(ws, 0, WS_USED, 0x00020000);
  float bb[2] = {bf2f(bsp[mi * 128 + 32 * wid + arow]), bf2f(bsp[mi * 128 + 32 * wid + arow + 4])};
  asm volatile("" : "+v"(bb[0]), "+v"(bb[1]), "+s"(tag));
  tag += EPOCH_STEP;
  f32x4 acc[2][7];
  auto zero_acc = [&]() {
#pragma unroll
    for (int m = 0; m < 2; ++m)
#pragma unroll
      for (int n = 0; n < 7; ++n) { acc[m][n] = (f32x4){0, 0, 0, 0}; asm volatile("" : "+v"(acc[m][n])); }
  };
  zero_acc();
  u32x2 z1p[2][7];
  const unsigned bt_off = OFF_BT + ((p * 64 + 4 * mi + wid) * 7) * 1024;
  GAS u64* f1 = (GAS u64*)(ws + OFF_F1) + p * 64 + 4 * mi + wid;
  if (G) {
    bf16x8 fa[2][2];
    bf16x4 fl[2][7], fh[2][7];
    const unsigned b0 = WR0 + (8 * fq + 4 * (fq & 1) + tq) * 224 + tp * 8;
    const unsigned b1 = WR0 + (8 * fq + 4 * (1 - (fq & 1)) + tq) * 224 + tp * 8;
    int slot = 0;
    auto rd = [&](int k, int s, int sl) {
      LAS char* lx = lds + ((ord >> (4 * s)) & 7) * 16384 + (k ? aoff1 : aoff0);
      LAS char* lw = lds + sl * WSL + k * 7168;
#pragma unroll
      for (int h = 0; h < 2; ++h) fa[k][h] = *(const LAS bf16x8*)(lx + h * 512);
#pragma unroll
      for (int n = 0; n < 7; ++n) { fl[k][n] = TRD(lw + b0 + n * 32); fh[k][n] = TRD(lw + b1 + n * 32); }
    };
    auto mm = [&](int k) {
#pragma unroll
      for (int n = 0; n < 7; ++n)
#pragma unroll
        for (int h = 0; h < 2; ++h) acc[h][n] = MFMA(CAT(fl[k][n], fh[k][n]), fa[k][h], acc[h][n]);
      SGB(0x8, 1); SGB(0x100, 2); SGB(0x8, 1); SGB(0x100, 2);
#pragma unroll
      for (int i = 0; i < 12; ++i) { SGB(0x8, 1); SGB(0x100, 1); }
      SCHED();
    };
    auto run = [&](int s0, int s1) {
#pragma unroll 1
      for (int s = s0; s < s1; ++s) {
        rd(1, s, slot); mm(0);
        slot = slot == 2 ? 0 : slot + 1;
        WAITL0(); BAR();
        rd(0, s + 1, slot); mm(1);
      }
    };
    BAR();
    STAMP(1);
    rd(0, 0, 0);
    SCHED();
    run(0, 7);
    STAMP(2);
    f32x4 z2[2][7];
    GAS u64* xch = (GAS u64*)(ws + OFF_X) + (mi * 4 + wid) * 8 * 64 + lane;
    const u64 tag40 = tag << 24;
    {
      float mean_p[2], m2_p[2];
#pragma unroll
      for (int h = 0; h < 2; ++h) {
        f32x4 s4 = {0, 0, 0, 0};
#pragma unroll
        for (int n = 0; n < 7; ++n) { z2[h][n] = gelu4(acc[h][n]); s4 += z2[h][n]; }
        mean_p[h] = xsum((s4[0] + s4[1]) + (s4[2] + s4[3])) * (1.0f / 112.0f);
      }
#pragma unroll
      for (int h = 0; h < 2; ++h) {
        f32x4 q4 = {0, 0, 0, 0};
#pragma unroll
        for (int n = 0; n < 7; ++n) { const f32x4 d = z2[h][n] - mean_p[h]; q4 += d * d; }
        m2_p[h] = xsum((q4[0] + q4[1]) + (q4[2] + q4[3]));
      }
      const float val = (fq & 2) ? ((fq & 1) ? m2_p[1] : m2_p[0]) : ((fq & 1) ? mean_p[1] : mean_p[0]);
      const unsigned bits = (__float_as_uint(val) + 0x80u) >> 8;
      __hip_atomic_store(xch + (p - 8) * 64, tag40 | bits, RLX_AGENT);
    }
    zero_acc();
    STAMP(3);
    run(7, 10);
    u64 gr[8];
#pragma unroll
    for (int c = 0; c < 8; ++c) gr[c] = __hip_atomic_load(xch + c * 64, RLX_AGENT);
    SCHED();
    run(10, 11);
    STAMP(4);
    for (unsigned spins = 0;; ++spins) {
      unsigned bh = 0, bl = 0;
#pragma unroll
      for (int c = 0; c < 8; ++c) {
        bh |= (unsigned)(gr[c] >> 32) ^ (unsigned)(tag40 >> 32);
        bl |= (unsigned)gr[c] ^ (unsigned)tag40;
      }
      if (__all((bh | (bl >> 24)) == 0) || spins > SPIN_CAP) break;
#pragma unroll
      for (int c = 0; c < 8; ++c) gr[c] = __hip_atomic_load(xch + c * 64, RLX_AGENT);
    }
    STAMP(5);
    {
      float pv[8], sum = 0.f, dev = 0.f;
#pragma unroll
      for (int c = 0; c < 8; ++c) { pv[c] = __uint_as_float((unsigned)gr[c] << 8); sum += pv[c]; }
      const float mean = sum * 0.125f;
#pragma unroll
      for (int c = 0; c < 8; ++c) { const float d = pv[c] - mean; dev = __builtin_fmaf(d, d, dev); }
      const float mine = (fq & 2) ? sum : dev * 112.0f;
      auto sm = __builtin_amdgcn_permlane32_swap(__float_as_uint(mine), __float_as_uint(mine), false, false);
      const float rstd = __builtin_amdgcn_rsqf((__uint_as_float(sm[0]) + __uint_as_float(sm[1])) * (1.0f / 896.0f) + 1e-5f);
      auto ml = __builtin_amdgcn_permlane32_swap(__float_as_uint(mean), __float_as_uint(mean), false, false);
      const float nm = -__uint_as_float(ml[0]) * rstd;
      auto rs = __builtin_amdgcn_permlane16_swap(__float_as_uint(rstd), __float_as_uint(rstd), false, false);
      auto mu = __builtin_amdgcn_permlane16_swap(__float_as_uint(nm), __float_as_uint(nm), false, false);
      LAS char* sc = lds + ((ord >> (4 * (wid >> 1))) & 7) * 16384 + (wid & 1) * 8192;
      const unsigned wsw = ((fi & 7) << 2) ^ (fi >> 2);
#pragma unroll
      for (int h = 0; h < 2; ++h)
#pragma unroll
        for (int n = 0; n < 7; ++n) {
          const f32x4 v = z2[h][n] * __uint_as_float(rs[h]) + __uint_as_float(mu[h]);
          const u32x2 o = {pk_bf16(v[0], v[1]), pk_bf16(v[2], v[3])};
          *(LAS u32x2*)(sc + (arow + 4 * h) * 256 + (((4 * n + fq) ^ wsw) << 3)) = o;
        }
      WAITL0();
      const unsigned ri = (fq << 2) | tq, rsw = ((ri & 7) << 2) ^ (ri >> 2);
#pragma unroll
      for (int n = 0; n < 7; ++n) {
        const unsigned a = (8 * fq + tq) * 256 + (((4 * n + tp) ^ rsw) << 3);
        const bf16x8 o = CAT(TRD(sc + a), TRD(sc + a + 1024));
        __builtin_amdgcn_raw_buffer_store_b128(*(const u32x4*)&o, rws, lane * 16, bt_off + n * 1024, 16);
      }
    }
    SCHED();
    STAMP(6);
    run(11, 13);
    WAITV(0);
    if (lane == 0) __hip_atomic_store(f1, tag, RLX_AGENT);
    STAMP(7);
    run(13, 14);
    STAMP(8);
#pragma unroll
    for (int h = 0; h < 2; ++h)
#pragma unroll
      for (int n = 0; n < 7; ++n) {
        const f32x4 v = gelu4(acc[h][n]);
        z1p[h][n] = (u32x2){pk_bf16(v[0], v[1]), pk_bf16(v[2], v[3])};
      }
  } else {
    bf16x8 fa[2][2], fb[2][7];
    const unsigned b0 = WR0 + fi * 128 + ((fq ^ (fi >> 1)) << 4);
    const unsigned b1 = WR0 + fi * 128 + (((fq + 4) ^ (fi >> 1)) << 4);
    int slot = 0;
    auto rd = [&](int k, int s, int sl) {
      LAS char* lx = lds + ((ord >> (4 * s)) & 7) * 16384 + (k ? aoff1 : aoff0);
      LAS char* lw = lds + sl * WSL + (k ? b1 : b0);
#pragma unroll
      for (int h = 0; h < 2; ++h) fa[k][h] = *(const LAS bf16x8*)(lx + h * 512);
#pragma unroll
      for (int n = 0; n < 7; ++n) fb[k][n] = *(const LAS bf16x8*)(lw + n * 2048);
    };
    auto mm = [&](int k) {
#pragma unroll
      for (int n = 0; n < 7; ++n)
#pragma unroll
        for (int h = 0; h < 2; ++h) acc[h][n] = MFMA(fa[k][h], fb[k][n], acc[h][n]);
#pragma unroll
      for (int i = 0; i < 9; ++i) { SGB(0x8, 1); SGB(0x100, 1); }
      SGB(0x8, 5);
      SCHED();
    };
    BAR();
    STAMP(1);
    rd(0, 0, 0);
    SCHED();
#pragma unroll 1
    for (int s = 0; s < 7; ++s) {
      rd(1, s, slot); mm(0);
      slot = slot == 2 ? 0 : slot + 1;
      WAITL0(); BAR();
      rd(0, s + 1, slot); mm(1);
    }
    STAMP(2);
#pragma unroll
    for (int n = 0; n < 7; ++n) {
      const u32x4 o = {pk_bf16(acc[0][n][0], acc[0][n][1]), pk_bf16(acc[0][n][2], acc[0][n][3]),
                       pk_bf16(acc[1][n][0], acc[1][n][1]), pk_bf16(acc[1][n][2], acc[1][n][3])};
      __builtin_amdgcn_raw_buffer_store_b128(o, rws, lane * 16, bt_off + n * 1024, 16);
    }
    STAMP(6);
    WAITV(0);
    if (lane == 0) __hip_atomic_store(f1, tag, RLX_AGENT);
    STAMP(7);
#pragma unroll
    for (int h = 0; h < 2; ++h)
#pragma unroll
      for (int n = 0; n < 7; ++n) z1p[h][n] = (u32x2){0x3f803f80u, 0x3f803f80u};
    bb[0] = bb[1] = 0.f;
  }
  zero_acc();
  {
    const unsigned boff = STA + lane * 16;
    bf16x8 fa[2][2][2], fb[2][2][7];
    auto rd = [&](int set, int slot) {
      LAS char* l = lds + slot * STG;
#pragma unroll
      for (int k = 0; k < 2; ++k) {
#pragma unroll
        for (int m = 0; m < 2; ++m) fa[set][k][m] = *(const LAS bf16x8*)(l + (k ? aoff1 : aoff0) + m * 512);
#pragma unroll
        for (int n = 0; n < 7; ++n) fb[set][k][n] = *(const LAS bf16x8*)(l + boff + k * 7168 + n * 1024);
      }
    };
    auto mm = [&](int set) {
#pragma unroll
      for (int k = 0; k < 2; ++k)
#pragma unroll
        for (int n = 0; n < 7; ++n)
#pragma unroll
          for (int m = 0; m < 2; ++m) acc[m][n] = MFMA(fb[set][k][n], fa[set][k][m], acc[m][n]);
    };
    auto sched = [&]() {
#pragma unroll
      for (int i = 0; i < 18; ++i) { SGB(0x8, 1); SGB(0x100, 1); }
      SGB(0x8, 10);
      SCHED();
    };
    STAMP(9);
    WAITL0();
    BAR();
    STAMP(10);
    rd(0, 0);
    int slot = 1;
#pragma unroll 1
    for (int t = 0; t < NT - 2; t += 2) {
      WAITL0(); BAR();
      rd(1, slot); mm(0); sched();
      slot = (slot + 1) & (RING - 1);
      WAITL0(); BAR();
      rd(0, slot); mm(1); sched();
      slot = (slot + 1) & (RING - 1);
    }
    WAITL0(); BAR();
    rd(1, slot); mm(0); sched();
    mm(1);
    SCHED();
  }
  STAMP(11);
  {
    const int odd = p & 1;
    u32x2 o[2][7];
#pragma unroll
    for (int m = 0; m < 2; ++m)
#pragma unroll
      for (int n = 0; n < 7; ++n) {
        const f32x4 z = {bflo(z1p[m][n][0]), bfhi(z1p[m][n][0]), bflo(z1p[m][n][1]), bfhi(z1p[m][n][1])};
        const f32x4 v = (acc[m][n] + bb[m]) * z;
        o[m][n] = (u32x2){pk_bf16(v[0], v[1]), pk_bf16(v[2], v[3])};
      }
    const unsigned o2 = OFF_O2 + ((mi * 8 + wid * 2) * 56 + ((7 * p) >> 1)) * 1024;
#pragma unroll
    for (int m = 0; m < 2; ++m) {
      const unsigned om = o2 + m * 56 * 1024;
      if (odd) {
        __builtin_amdgcn_raw_buffer_store_b64(o[m][0], rws, lane * 16 + 8, om, 16);
#pragma unroll
        for (int k = 0; k < 3; ++k)
          __builtin_amdgcn_raw_buffer_store_b128((u32x4){o[m][2 * k + 1][0], o[m][2 * k + 1][1], o[m][2 * k + 2][0], o[m][2 * k + 2][1]},
                                                 rws, lane * 16, om + (k + 1) * 1024, 16);
      } else {
#pragma unroll
        for (int k = 0; k < 3; ++k)
          __builtin_amdgcn_raw_buffer_store_b128((u32x4){o[m][2 * k][0], o[m][2 * k][1], o[m][2 * k + 1][0], o[m][2 * k + 1][1]},
                                                 rws, lane * 16, om + k * 1024, 16);
        __builtin_amdgcn_raw_buffer_store_b64(o[m][6], rws, lane * 16, om + 3 * 1024, 16);
      }
    }
  }
  STAMP(12);
  WAITV(0);
  if (lane == 0) __hip_atomic_store((GAS u64*)(ws + OFF_F2) + (4 * mi + wid) * 16 + p, tag, RLX_AGENT);
  STAMP(13);
  BAR();
  if (r >= 28) { STAMP_FLUSH(); return; }
  const int mt = 4 * rg + wid;
  unsigned wb[4];
#pragma unroll
  for (int n = 0; n < 4; ++n) wb[n] = (4 * fq + tq) * 128 + ((n ^ (((4 * fq + tq) >> 1) & 3)) << 5) + tp * 8;
  bf16x4 wl[2][16], wh[2][16];
  auto rdw = [&](int c) {
#pragma unroll
    for (int i = 0; i < 16; ++i) {
      LAS char* a = lds + wb[i & 3] + (wo_loc(c) + (i >> 2) * 4096);
      wl[c & 1][i] = TRD(a);
      wh[c & 1][i] = TRD(a + 2048);
    }
  };
  rdw(0);
  SCHED();
  wait_flags((GAS u64*)(ws + OFF_F2) + (mt >> 1) * 16 + fi, tag);
  STAMP(14);
  if (b == 0 && threadIdx.x == 0) __hip_atomic_store((GAS u64*)(ws + OFF_E), tag, RLX_AGENT);
  const unsigned o2 = OFF_O2 + mt * 56 * 1024;
  u32x4 hf[7][4];
  auto ldh = [&](int c) {
    int cc = c + dg;
    if (cc >= 7) cc -= 7;
#pragma unroll
    for (int k = 0; k < 4; ++k) hf[c][k] = __builtin_amdgcn_raw_buffer_load_b128(rws, lane * 16 + k * 1024, o2 + (28 + cc * 4) * 1024, 16);
  };
  ldh(0); ldh(1); ldh(2); ldh(3);
  SCHED();
  u32x4 yr[2], yi[2];
  float g;
  const int t = 32 * (mt >> 1) + arow + 4 * (mt & 1);
  f32x4 oc[4];
#pragma unroll
  for (int n = 0; n < 4; ++n) oc[n] = (f32x4){0, 0, 0, 0};
#pragma unroll
  for (int c = 0; c < 7; ++c) {
    if (c < 3) ldh(c + 4);
    if (c == 3) {
#pragma unroll
      for (int k = 0; k < 2; ++k) {
        yr[k] = __builtin_amdgcn_raw_buffer_load_b128(rws, lane * 16 + k * 1024, o2 + 2 * dg * 1024, 16);
        yi[k] = __builtin_amdgcn_raw_buffer_load_b128(rws, lane * 16 + k * 1024, o2 + (14 + 2 * dg) * 1024, 16);
      }
      g = __hip_atomic_load((GAS float*)(ws + OFF_G) + t, RLX_AGENT);
    }
    SCHED();
    if (c < 6) rdw(c + 1);
#pragma unroll
    for (int i = 0; i < 16; ++i) oc[i & 3] = MFMA(CAT(wl[c & 1][i], wh[c & 1][i]), *(const bf16x8*)&hf[c][i >> 2], oc[i & 3]);
    if (c < 6) {
#pragma unroll
      for (int i = 0; i < 16; ++i) { SGB(0x8, 1); SGB(0x100, 2); }
    }
    SCHED();
  }
  STAMP(15);
  const float g1 = 1.0f - g;
#pragma unroll
  for (int n = 0; n < 4; ++n) {
    const unsigned r0 = yr[n >> 1][(n & 1) * 2], r1 = yr[n >> 1][(n & 1) * 2 + 1];
    const unsigned i0 = yi[n >> 1][(n & 1) * 2], i1 = yi[n >> 1][(n & 1) * 2 + 1];
    const f32x4 y = {bflo(r0) - bflo(i0), bfhi(r0) - bfhi(i0), bflo(r1) - bflo(i1), bfhi(r1) - bfhi(i1)};
    const f32x4 v = y * g + oc[n] * g1;
    *(u32x2*)(out + (size_t)t * D + 64 * dg + 16 * n + 4 * fq) = (u32x2){pk_bf16(v[0], v[1]), pk_bf16(v[2], v[3])};
  }
  STAMP(16);
  STAMP_FLUSH();
}

__global__ void tiny_mark(u64* p) { if (threadIdx.x == 0) __hip_atomic_store((GAS u64*)p, (u64)__builtin_amdgcn_s_memrealtime(), RLX_AGENT); }
__global__ void tiny_wait(u64* p) { const u64 t = __hip_atomic_load((GAS u64*)p, RLX_AGENT); const u64 n = __builtin_amdgcn_s_memrealtime();
  if (n - t < 20000) while (__builtin_amdgcn_s_memrealtime() < t + 10000) __builtin_amdgcn_s_sleep(1); }
extern "C" void kernel_launch(void* const* d_in, const int* in_sizes, int n_in, void* d_out, int out_size, void* d_ws,
                              size_t ws_size, hipStream_t stream) {
  tiny_mark<<<1, 64, 0, stream>>>((u64*)((char*)d_ws + WS_USED));
  fused<<<256, 512, 0, stream>>>((const u16*)d_in[0], (const u16*)d_in[1], (const u16*)d_in[2], (const u16*)d_in[3],
                                 (const u16*)d_in[4], (const u16*)d_in[6], (const u16*)d_in[7], (const u16*)d_in[8],
                                 (const u16*)d_in[9], (const u16*)d_in[10], (char*)d_ws, (u16*)d_out);
}
```

```cpp
#include <hip/hip_runtime.h>
#include <hip/hip_bf16.h>
#include <type_traits>

typedef unsigned short u16;
typedef unsigned long long u64;
typedef short bf16x8 __attribute__((ext_vector_type(8)));
typedef short bf16x4 __attribute__((ext_vector_type(4)));
typedef float f32x2 __attribute__((ext_vector_type(2)));
typedef float f32x4 __attribute__((ext_vector_type(4)));
typedef unsigned u32x2 __attribute__((ext_vector_type(2)));
typedef unsigned u32x4 __attribute__((ext_vector_type(4)));
#define LAS __attribute__((address_space(3)))
#define GAS __attribute__((address_space(1)))
#define RLX_AGENT __ATOMIC_RELAXED, __HIP_MEMORY_SCOPE_AGENT

#define WAITV(n) asm volatile("s_waitcnt vmcnt(%0)" ::"n"(n) : "memory")
#define WAITL0() do { asm volatile("" ::: "memory"); __builtin_amdgcn_s_waitcnt(0xC07F); asm volatile("" ::: "memory"); } while (0)
#define BAR() do { asm volatile("" ::: "memory"); __builtin_amdgcn_s_barrier(); asm volatile("" ::: "memory"); } while (0)
#define SCHED() __builtin_amdgcn_sched_barrier(0)
#define SGB(mask, n) __builtin_amdgcn_sched_group_barrier(mask, n, 0)
#define MFMA(a, b, c) __builtin_amdgcn_mfma_f32_16x16x32_bf16(a, b, c, 0, 0, 0)
#define TRD(p) __builtin_amdgcn_ds_read_tr16_b64_v4i16((LAS bf16x4*)(p))
#define CAT(lo, hi) __builtin_shufflevector(lo, hi, 0, 1, 2, 3, 4, 5, 6, 7)

template <bool SC1 = false>
__device__ __forceinline__ void glds(const void* sbase, unsigned voff, unsigned l) {
  l = __builtin_amdgcn_readfirstlane(l);
  if (SC1) asm volatile("s_mov_b32 m0, %2\n\ts_nop 0\n\tglobal_load_lds_dwordx4 %0, %1 sc1" :: "v"(voff), "s"(sbase), "s"(l) : "memory");
  else asm volatile("s_mov_b32 m0, %2\n\ts_nop 0\n\tglobal_load_lds_dwordx4 %0, %1" :: "v"(voff), "s"(sbase), "s"(l) : "memory");
}
__device__ __forceinline__ void touch(const void* sbase, unsigned voff, unsigned l) {
  l = __builtin_amdgcn_readfirstlane(l);
  asm volatile("s_mov_b32 m0, %2\n\ts_nop 0\n\tglobal_load_lds_dword %0, %1" :: "v"(voff), "s"(sbase), "s"(l) : "memory");
}
#define PWA 2
#define PWB 4
#define PWBS 12
#define NOZ1 0
#define PWW 2
#define PWR 0
#define NSG 1
#define NSP 3
#define GSLEEP 16
#define ASLEEP 0
#define BSLEEP 8
#define FLAGKS 5
#define CODE_KB 30
#define BSC1 true
constexpr int S = 2048, D = 448;

__device__ __forceinline__ float bf2f(u16 v) { return __uint_as_float(((unsigned)v) << 16); }
__device__ __forceinline__ float bflo(unsigned v) { return __uint_as_float(v << 16); }
__device__ __forceinline__ float bfhi(unsigned v) { return __uint_as_float(v & 0xffff0000u); }
__device__ __forceinline__ unsigned pk_bf16(float lo, float hi) {
  typedef __bf16 bf2 __attribute__((ext_vector_type(2)));
  f32x2 v = {lo, hi};
  bf2 b = __builtin_convertvector(v, bf2);
  return *(unsigned*)&b;
}
__device__ __forceinline__ f32x2 gelu2(f32x2 v) {
  const float c0 = -2.0f * 0.7978845608028654f * 1.4426950408889634f;
  const float c1 = c0 * 0.044715f;
  const f32x2 u = v * ((v * v) * c1 + c0);
  f32x2 e = {__builtin_amdgcn_exp2f(u.x), __builtin_amdgcn_exp2f(u.y)};
  e = e + 1.0f;
  const f32x2 rc = {__builtin_amdgcn_rcpf(e.x), __builtin_amdgcn_rcpf(e.y)};
  return v * rc;
}
__device__ __forceinline__ f32x4 gelu4(f32x4 v) {
  const f32x2 lo = gelu2((f32x2){v[0], v[1]}), hi = gelu2((f32x2){v[2], v[3]});
  return (f32x4){lo.x, lo.y, hi.x, hi.y};
}
__device__ __forceinline__ float xsum(float s) {
  auto a = __builtin_amdgcn_permlane16_swap(__float_as_uint(s), __float_as_uint(s), false, false);
  s = __uint_as_float(a[0]) + __uint_as_float(a[1]);
  auto b = __builtin_amdgcn_permlane32_swap(__float_as_uint(s), __float_as_uint(s), false, false);
  return __uint_as_float(b[0]) + __uint_as_float(b[1]);
}

constexpr unsigned OFF_BT = 0;
constexpr unsigned OFF_O2 = OFF_BT + 1792 * S * 2;
constexpr unsigned OFF_Z1 = OFF_O2 + 1792 * S * 2;
constexpr unsigned OFF_G = OFF_Z1 + 896 * S * 2;
constexpr unsigned OFF_X = OFF_G + 8192;
constexpr unsigned OFF_F1 = OFF_X + 64 * 14 * 512;
constexpr unsigned OFF_F2 = OFF_F1 + 256 * 128;
constexpr unsigned OFF_F3 = OFF_F2 + 256 * 128;
constexpr unsigned OFF_E = OFF_F3 + 256 * 128;
constexpr unsigned WS_USED = OFF_E + 128;
constexpr u64 EPOCH_STEP = 0x9E3779B97F4A7C15ull;
constexpr unsigned SPIN_CAP = 1u << 20;

#define CSTAMP(i)
#define STAMP_DECL
#define STAMP(i)
#define STAMP_FLUSH()

constexpr unsigned R1 = 89600, R1S = 24576, SCR = 89600, W1 = 105984;
constexpr int STA = 16384, STB = 14336, STG = STA + STB, RING = 4, NT = 32, LEAD = 4;
constexpr unsigned SPARE = RING * STG;
constexpr unsigned W1C0 = 77824;
constexpr unsigned STAT = 86016, FLG = 155648, CNT = 163328, JUNK = 163584;
__device__ __forceinline__ constexpr unsigned wo_loc(int c) { return c < 5 ? c * 16384 : 90112 + (c - 5) * 16384; }

__device__ __forceinline__ void st128(__amdgpu_buffer_rsrc_t rs, u32x4 v, unsigned off) {
  __builtin_amdgcn_raw_buffer_store_b128(v, rs, off, 0, 16);
}
__device__ __forceinline__ void wait_flags(GAS u64* p, u64 tag) {
#pragma unroll 1
  for (unsigned spins = 0; spins < SPIN_CAP; ++spins) {
    const u64 v = __hip_atomic_load(p, RLX_AGENT);
    if (__all(v == tag)) break;
    __builtin_amdgcn_s_sleep(2);
  }
  __builtin_amdgcn_fence(__ATOMIC_ACQUIRE, "wavefront");
}

__device__ __forceinline__ void arrive_and_flag(LAS char* lds, int which, char* ws, unsigned flag_off, u64 tag, int lane) {
  WAITV(0);
  if (lane == 0) {
    const unsigned old = __hip_atomic_fetch_add((LAS unsigned*)(lds + CNT) + which, 1u, __ATOMIC_RELAXED, __HIP_MEMORY_SCOPE_WORKGROUP);
    if (old == 3) __hip_atomic_store((GAS u64*)(ws + flag_off), tag, RLX_AGENT);
  }
}

struct Loader2 {
  const char* base; unsigned step, l0; int cls, nxt, slot;
  template <int P, bool SC1> __device__ __forceinline__ void issue(const unsigned (&off)[P]) {
    const int st = ((nxt & 3) == cls) ? ((nxt + LEAD) & (NT - 1)) : nxt;
    const char* g = base + (size_t)st * step;
    const unsigned l = l0 + slot * STG;
#pragma unroll
    for (int i = 0; i < P; ++i) glds<SC1>(g, off[i], l + i * 1024);
    ++nxt;
    slot = (slot + 1) & (RING - 1);
  }
};

__global__ __launch_bounds__(512, 2) void fused(const u16* __restrict__ x, const u16* __restrict__ wsr, const u16* __restrict__ wsi,
                                                const u16* __restrict__ wdr, const u16* __restrict__ wdi, const u16* __restrict__ win,
                                                const u16* __restrict__ wsp, const u16* __restrict__ bsp, const u16* __restrict__ wout,
                                                const u16* __restrict__ gw, char* __restrict__ ws, u16* __restrict__ out) {
  __shared__ __attribute__((aligned(1024))) char smem[163840];
  LAS char* lds = (LAS char*)smem;
  const int wid = __builtin_amdgcn_readfirstlane(threadIdx.x >> 6), lane = threadIdx.x & 63;
  STAMP_DECL; STAMP(0);
  const int b = blockIdx.x, xcd = b & 7, r = b >> 3;
  const bool G = xcd < 4;
  const int ib = G ? (r & 3) : (r & 7), ia = G ? (r >> 2) : (r >> 3);
  const int mi = (G ? xcd * 4 : (xcd & 1) * 8) + ib;
  const int p = (G ? 8 : (xcd & 2) * 2) + ia;
  const int cls = (ia + ib) & 3;
  const bool spare = (p & 11) == 3;
  const int zg = spare ? 14 + (p >> 2) : p - (p > 3) - (p > 7);
  const unsigned o7 = (cls & 2) ? ((cls & 1) ? 0x6423501u : 0x6542031u) : ((cls & 1) ? 0x6532410u : 0x5463120u);
  const unsigned ldsb = __builtin_amdgcn_readfirstlane((unsigned)(size_t)lds);
  if (!spare) {
    const unsigned l3 = lane >> 3, l7 = lane & 7, l4 = (lane >> 4) & 1;
    const char* xg = (const char*)x + (size_t)(mi * 128 + wid * 16) * 896;
    const unsigned xo0 = l3 * 896 + ((l7 ^ (l4 | (((2 * wid) & 3) << 1))) << 4);
    const unsigned xo1 = (xo0 ^ 32) + 8 * 896;
    const char* wg = (const char*)win + (size_t)(wid * 8) * 3584 + 1792 + zg * 128;
    const unsigned wo = l3 * 3584 + (((((l7 >> 1) ^ (l4 | ((wid & 1) << 1))) << 1) | (l7 & 1)) << 4);
#pragma unroll
    for (int s = 0; s < 2; ++s) {
      const unsigned st = (o7 >> (4 * s)) & 7;
      glds(xg + st * 128, xo0, ldsb + R1 + s * R1S + wid * 2048);
      glds(xg + st * 128, xo1, ldsb + R1 + s * R1S + wid * 2048 + 1024);
      glds(wg + st * (64 * 3584), wo, ldsb + R1 + s * R1S + 16384 + wid * 1024);
    }
  }
  if (wid == 7 && CODE_KB > 0) {
    u64 pc;
    asm volatile("s_getpc_b64 %0" : "=s"(pc));
    const unsigned o = (r * 8 + (lane & 7)) * 128;
    touch((const char*)(pc & ~127ull), o < CODE_KB * 1024 ? o : 0, ldsb + JUNK);
  }
  u64 tag = *(const __attribute__((address_space(4))) u64*)(ws + OFF_E);
  const int rg = 4 * xcd + (r & 3), dg = r >> 2, rot3 = (dg + 2 * (r & 3)) % 7;

  if (wid >= 4) {
    const int lw = wid - 4;
    int lane = threadIdx.x;
    asm volatile("" : "+v"(lane));
    lane &= 63;
    const unsigned l3 = lane >> 3, l7 = lane & 7, l4 = (lane >> 4) & 1;
    const u16* am = p < 4 ? wsr : wsi;
    am = G ? wsp : am;
    asm volatile("" : "+s"(tag));
    tag += EPOCH_STEP;
    const unsigned pwo = (lane >> 2) * 4096 + (lane & 3) * 128;
    if (spare) {
      BAR();
      touch(wout, (unsigned)(((mi * 2 + (p >> 2)) * 4 + lw) * 49 + (lane < 49 ? lane : 48)) * 128, ldsb + JUNK);
      const char* pw = (const char*)am + (size_t)(mi * 128 + lw * 32) * 4096;
#pragma unroll 1
      for (int j = 0; j < NSP; ++j) {
        touch(pw + j * 512, pwo, ldsb + JUNK);
        touch(pw + j * 512 + 16 * 4096, pwo, ldsb + JUNK);
      }
      const char* ps = (const char*)wsp + (size_t)((mi * 2 + (p >> 2)) * 64 + lw * 16) * 4096 + PWA * 512;
#pragma unroll 1
      for (int j = 0; j < NSG; ++j) touch(ps + j * 512, pwo, ldsb + JUNK);
      WAITV(0);
      STAMP(6);
    } else {
      const char* xg = (const char*)x + (size_t)(mi * 128 + lw * 32) * 896;
      const unsigned xo = l3 * 896 + ((l7 ^ l4) << 4);
      const char* wg = (const char*)win + (size_t)(lw * 16) * 3584 + zg * 128;
      const unsigned wo = l3 * 3584 + (((((l7 >> 1) ^ l4) << 1) | (l7 & 1)) << 4);
      STAMP(1);
      WAITV(3);
      BAR();
      int slot = 2;
#pragma unroll 1
      for (int s = 2; s < 7; ++s) {
        const unsigned st = (o7 >> (4 * s)) & 7, l = ldsb + R1 + slot * R1S;
#pragma unroll
        for (int i = 0; i < 4; ++i) glds(xg + st * 128 + i * (8 * 896), xo ^ (i << 5), l + lw * 4096 + i * 1024);
#pragma unroll
        for (int i = 0; i < 2; ++i) glds(wg + st * (64 * 3584) + i * (8 * 3584) + 1792, wo ^ (i << 6), l + 16384 + lw * 2048 + i * 1024);
        slot = slot == 2 ? 0 : slot + 1;
        WAITV(6);
        BAR();
      }
      const u16* dm = zg < 7 ? wdr : wdi;
      const char* dg_ = (const char*)dm + (size_t)((zg < 7 ? zg : zg - 7) * 64 + lw * 16) * 896;
      const unsigned do_ = l3 * 896 + ((l7 ^ (l3 >> 1)) << 4);
      auto dchunk = [&](int c) {
        const unsigned st = (o7 >> (4 * c)) & 7;
#pragma unroll
        for (int i = 0; i < 2; ++i) glds(dg_ + st * 128 + i * (8 * 896), do_ ^ (i << 6), ldsb + c * 8192 + lw * 2048 + i * 1024);
      };
      auto wchunk = [&](int c) {
        const unsigned st = (o7 >> (4 * c)) & 7;
#pragma unroll
        for (int i = 0; i < 2; ++i)
          glds(wg + st * (64 * 3584) + i * (8 * 3584), wo ^ (i << 6), ldsb + (c ? W1 + c * 8192 : W1C0) + lw * 2048 + i * 1024);
      };
      dchunk(0); dchunk(1); dchunk(2);
      WAITV(6);
      BAR();
#pragma unroll 1
      for (int c = 3; c < 7; ++c) dchunk(c);
#pragma unroll 1
      for (int c = 0; c < 7; ++c) wchunk(c);
      const char* pw = (const char*)am + (size_t)(mi * 128) * 4096;
      auto warm = [&](int j) {
        if (G) { if (j < 8) touch(pw + (size_t)ia * 16 * 4096 + j * 512, pwo, ldsb + JUNK); }
        else { const int rw = ia * 3 + j % 3, bl = NSP + j / 3; if (rw < 8 && bl < 8) touch(pw + (size_t)rw * 16 * 4096 + bl * 512, pwo, ldsb + JUNK); }
      };
      WAITV(14);
      STAMP(2);
      BAR();
      if (G && lw < PWA) warm(lw);
      __builtin_amdgcn_s_sleep(GSLEEP);
      GAS u64* xch = (GAS u64*)(ws + OFF_X) + (4 * mi + lw) * 14 * 64 + lane;
      const u64 tag40 = tag << 24;
      u64 gr[14];
#pragma unroll 1
      for (unsigned spins = 0; spins < SPIN_CAP; ++spins) {
        unsigned bh = 0, bl = 0;
#pragma unroll
        for (int c = 0; c < 14; ++c) gr[c] = __hip_atomic_load(xch + c * 64, RLX_AGENT);
#pragma unroll
        for (int c = 0; c < 14; ++c) {
          bh |= (unsigned)(gr[c] >> 32) ^ (unsigned)(tag40 >> 32);
          bl |= (unsigned)gr[c] ^ (unsigned)tag40;
        }
        if (__all((bh | (bl >> 24)) == 0)) break;
      }
      float pv[14], sum = 0.f, dev = 0.f;
#pragma unroll
      for (int c = 0; c < 14; ++c) { pv[c] = __uint_as_float((unsigned)gr[c] << 8); sum += pv[c]; }
      const float mean = sum * (1.0f / 14.0f);
#pragma unroll
      for (int c = 0; c < 14; ++c) { const float d = pv[c] - mean; dev = __builtin_fmaf(d, d, dev); }
      const float mine = lane >= 32 ? sum : dev * 64.0f;
      auto sm = __builtin_amdgcn_permlane32_swap(__float_as_uint(mine), __float_as_uint(mine), false, false);
      const float rstd = __builtin_amdgcn_rsqf((__uint_as_float(sm[0]) + __uint_as_float(sm[1])) * (1.0f / 896.0f) + 1e-5f);
      if (lane < 32) *(LAS f32x2*)(lds + STAT + lw * 256 + lane * 8) = (f32x2){rstd, -mean * rstd};
      WAITV(0);
      WAITL0();
      STAMP(6);
      BAR();
      if (lw < PWW) {
#pragma unroll 1
        for (int j = 0; j < PWR; ++j) { warm((G ? PWA : 0) + j * PWW + lw); WAITV(0); }
      }
#pragma unroll 1
      for (unsigned spins = 0; spins < SPIN_CAP; ++spins) {
        if (__hip_atomic_load((LAS unsigned*)(lds + CNT), __ATOMIC_RELAXED, __HIP_MEMORY_SCOPE_WORKGROUP) == 4) break;
        __builtin_amdgcn_s_sleep(1);
      }
      STAMP(7);
    }
    const unsigned wo_off = l3 * 896 + (((((l7 >> 1) ^ ((lane >> 4) & 3)) << 1) | (l7 & 1)) << 4);
    const char* wo_base = (const char*)wout + (dg < 7 ? dg : 0) * 128 + (size_t)lw * 32 * 896;
    auto wo_chunk = [&](int c) {
      int cc = c + rot3;
      if (cc >= 7) cc -= 7;
#pragma unroll
      for (int i = 0; i < 4; ++i) glds(wo_base + (size_t)(cc * 128 + i * 8) * 896, wo_off, ldsb + wo_loc(c) + lw * 4096 + i * 1024);
    };
    const int pj = p & 7, g0 = (7 * pj) >> 2, g1 = (7 * pj + 6) >> 2;
    const unsigned fo = OFF_F3 + (mi * 16 + min(g0 + ((lane >> 1) & 3), g1)) * 128;
    const int tflag = (G && lw == 0) ? 8 + cls : -1, tz = G ? 12 + 4 * ib + cls : -1;
    auto z1_tile = [&]() {
      const unsigned v = *(const LAS unsigned*)(lds + FLG + lane * 4);
      if (!__all(v == (unsigned)(tag >> ((lane & 1) * 32)))) wait_flags((GAS u64*)(ws + fo), tag);
      const char* zb = ws + OFF_Z1 + (size_t)(2 * (4 * mi + lw) * 28 + ((7 * pj) >> 1)) * 1024;
#pragma unroll
      for (int i = 0; i < 8; ++i) glds<true>(zb + ((i >> 2) * 28 + (i & 3)) * 1024, lane * 16, ldsb + SPARE + lw * 8192 + i * 1024);
    };
    if (lw < 2) {
      unsigned off[8];
#pragma unroll
      for (int i = 0; i < 8; ++i) off[i] = (8 * i + l3) * 4096 + ((l7 ^ (l4 | ((i & 3) << 1))) << 4);
      Loader2 L{(const char*)am + (size_t)(mi * 128 + lw * 64) * 4096, 128, ldsb + lw * 8192, cls, 0, 0};
#pragma unroll 1
      for (int t = 0; t < 3; ++t) L.issue<8, false>(off);
      WAITV(16);
      STAMP(3);
      BAR();
      L.issue<8, false>(off);
#pragma unroll 1
      for (int t = 0; t < NT - 4; ++t) {
        WAITV(16);
        BAR();
        if (t == tflag)
          asm volatile("s_mov_b32 m0, %2\n\ts_nop 0\n\tglobal_load_lds_dword %0, %1 sc1" :: "v"(fo + (lane & 1) * 4), "s"(ws), "s"(ldsb + FLG) : "memory");
        L.issue<8, false>(off);
        if (t == tz) z1_tile();
      }
      WAITV(16); BAR();
      wo_chunk(0);
      WAITV(12); BAR();
    } else {
      const int l2 = lw - 2;
      unsigned off[7];
      const int g2 = g1 - g0 == 2 ? g0 + 1 : (pj & 1 ? 14 + (pj >> 2) : g0);
      const int qr = lane < 16 ? g0 : (lane < 32 ? g1 : g2);
#pragma unroll
      for (int i = 0; i < 7; ++i) off[i] = i * 1024 + lane * 16;
      Loader2 L{ws + OFF_BT + (size_t)p * 64 * 7 * 1024 + l2 * 7168, STB, ldsb + STA + l2 * 7168, cls, 0, 0};
      __builtin_amdgcn_s_sleep(BSLEEP);
      wait_flags((GAS u64*)(ws + OFF_F1 + (qr * 16 + (lane & 15)) * 128), tag);
      STAMP(4);
      L.issue<7, BSC1>(off);
      L.issue<7, BSC1>(off);
      WAITV(7);
      STAMP(3);
      BAR();
      L.issue<7, BSC1>(off);
      L.issue<7, BSC1>(off);
#pragma unroll 1
      for (int t = 0; t < NT - 4; ++t) {
        WAITV(14);
        BAR();
        L.issue<7, BSC1>(off);
        if (t == tz) z1_tile();
      }
      WAITV(14); BAR();
      wo_chunk(0);
      WAITV(11); BAR();
    }
    wo_chunk(1); wo_chunk(2);
    WAITV(12);
    BAR();
    wo_chunk(3); wo_chunk(4);
    BAR();
    wo_chunk(5); wo_chunk(6);
    WAITV(0);
    STAMP(5);
    BAR();
    if (lw == 0 && r < 28) {
      wait_flags((GAS u64*)(ws + OFF_F2 + ((rg >> 1) * 16 + (lane & 15)) * 128), tag);
      if (b == 0 && lane == 0) __hip_atomic_store((GAS u64*)(ws + OFF_E), tag, RLX_AGENT);
      BAR();
    }
    STAMP_FLUSH();
    return;
  }

  const int fi = lane & 15, fq = lane >> 4;
  const unsigned arow = 8 * (fi >> 2) + (fi & 3);
  const unsigned tq = fi >> 2, tp = lane & 3;
  const unsigned aoff0 = (32 * wid + arow) * 128 + ((fq ^ (fi >> 1)) << 4);
  const unsigned aoff1 = aoff0 ^ 64;
  const __amdgpu_buffer_rsrc_t rws = __builtin_amdgcn_make_buffer_rsrc(ws, 0, WS_USED, 0x00020000);
  asm volatile("" : "+s"(tag));
  tag += EPOCH_STEP;
  const int kb = 4 * mi + wid;
  unsigned braw[2] = {0, 0};
  u32x4 z1o[2][2];
  if (threadIdx.x < 3) ((LAS unsigned*)(lds + CNT))[threadIdx.x] = 0;
  if (spare) {
    const int lc = lane < 56 ? lane : 55;
    const int row0 = mi * 128 + (p >> 2) * 64 + wid * 16;
    const u32x4 gv = *(const u32x4*)(gw + lc * 8);
    u32x4 xv[16];
#pragma unroll
    for (int i = 0; i < 16; ++i) xv[i] = *(const u32x4*)(x + (size_t)(row0 + i) * D + lc * 8);
    SCHED();
    WAITL0();
    BAR();
    if (threadIdx.x == 0) __hip_atomic_store((GAS u64*)(ws + OFF_F1 + (zg * 16 + mi) * 128), tag, RLX_AGENT);
    float res = 0.f;
#pragma unroll
    for (int i = 0; i < 16; ++i) {
      float a = 0.f;
#pragma unroll
      for (int j = 0; j < 4; ++j) a += bflo(xv[i][j]) * bflo(gv[j]) + bfhi(xv[i][j]) * bfhi(gv[j]);
      a = lane < 56 ? a : 0.f;
      a = xsum(a);
      a += __builtin_bit_cast(float, __builtin_amdgcn_update_dpp(0, __builtin_bit_cast(int, a), 0x128, 0xf, 0xf, false));
      a += __builtin_bit_cast(float, __builtin_amdgcn_update_dpp(0, __builtin_bit_cast(int, a), 0x124, 0xf, 0xf, false));
      a += __builtin_bit_cast(float, __builtin_amdgcn_update_dpp(0, __builtin_bit_cast(int, a), 0x122, 0xf, 0xf, false));
      a += __builtin_bit_cast(float, __builtin_amdgcn_update_dpp(0, __builtin_bit_cast(int, a), 0x121, 0xf, 0xf, false));
      if (lane == i) res = a;
    }
    if (lane < 16) __hip_atomic_store((GAS float*)(ws + OFF_G) + row0 + lane, 1.0f / (1.0f + __expf(-res)), RLX_AGENT);
#pragma unroll
    for (int h = 0; h < 2; ++h) z1o[h][0] = z1o[h][1] = (u32x4){0, 0, 0, 0};
  } else {
    bf16x8 xf[14][2];
    f32x4 z2[2][4], ac[2][4];
    const unsigned fw = ((tq >> 1) & 1) | ((fq & 1) << 1);
    unsigned bw[4];
#pragma unroll
    for (int n = 0; n < 4; ++n) bw[n] = R1 + 16384 + (8 * fq + tq) * 128 + ((n ^ fw) << 5) + tp * 8;
    const unsigned ax0 = R1 + aoff0, ax1 = R1 + aoff1;
    {
      bf16x8 wf[2][2][4];
      auto rd = [&](int t) {
        const unsigned so = (t % 3) * R1S;
#pragma unroll
        for (int k = 0; k < 2; ++k) {
#pragma unroll
          for (int h = 0; h < 2; ++h) xf[2 * t + k][h] = *(const LAS bf16x8*)(lds + (k ? ax1 : ax0) + so + h * 512);
#pragma unroll
          for (int n = 0; n < 4; ++n) wf[t & 1][k][n] = CAT(TRD(lds + bw[n] + so + k * 4096), TRD(lds + bw[n] + so + k * 4096 + 512));
        }
      };
      auto mm = [&](int t) {
#pragma unroll
        for (int k = 0; k < 2; ++k)
#pragma unroll
          for (int n = 0; n < 4; ++n)
#pragma unroll
            for (int h = 0; h < 2; ++h) z2[h][n] = MFMA(wf[t & 1][k][n], xf[2 * t + k][h], z2[h][n]);
      };
#pragma unroll
      for (int h = 0; h < 2; ++h)
#pragma unroll
        for (int n = 0; n < 4; ++n) z2[h][n] = (f32x4){0, 0, 0, 0};
      WAITV(3);
      WAITL0();
      BAR();
      STAMP(1);
      rd(0);
      WAITV(0);
      braw[0] = bsp[mi * 128 + 32 * wid + arow];
      braw[1] = bsp[mi * 128 + 32 * wid + arow + 4];
#pragma unroll
      for (int t = 0; t < 6; ++t) {
        SCHED();
        WAITL0(); BAR();
        rd(t + 1); mm(t);
#pragma unroll
        for (int i = 0; i < 10; ++i) { SGB(0x8, 1); SGB(0x100, 2); }
        SGB(0x8, 6);
      }
      SCHED();
      mm(6);
    }
    STAMP(2);
    {
      float mean_p[2], m2_p[2];
#pragma unroll
      for (int h = 0; h < 2; ++h) {
        f32x4 s4 = {0, 0, 0, 0};
#pragma unroll
        for (int n = 0; n < 4; ++n) { z2[h][n] = gelu4(z2[h][n]); s4 += z2[h][n]; }
        mean_p[h] = xsum((s4[0] + s4[1]) + (s4[2] + s4[3])) * (1.0f / 64.0f);
      }
#pragma unroll
      for (int h = 0; h < 2; ++h) {
        f32x4 q4 = {0, 0, 0, 0};
#pragma unroll
        for (int n = 0; n < 4; ++n) { const f32x4 d = z2[h][n] - mean_p[h]; q4 += d * d; }
        m2_p[h] = xsum((q4[0] + q4[1]) + (q4[2] + q4[3]));
      }
      const float val = (fq & 2) ? ((fq & 1) ? m2_p[1] : m2_p[0]) : ((fq & 1) ? mean_p[1] : mean_p[0]);
      const unsigned bits = (__float_as_uint(val) + 0x80u) >> 8;
      asm volatile("" : "+v"(braw[0]), "+v"(braw[1]));
      __hip_atomic_store((GAS u64*)(ws + OFF_X) + (kb * 14 + zg) * 64 + lane, (tag << 24) | bits, RLX_AGENT);
    }
    SCHED();
    STAMP(3);
    BAR();
    STAMP(4);
    {
      bf16x8 df[14][4];
      const unsigned d0 = fi * 128 + ((fq ^ (fi >> 1)) << 4), d1 = d0 ^ 64;
      auto rdd = [&](int ks) {
#pragma unroll
        for (int n = 0; n < 4; ++n) df[ks][n] = *(const LAS bf16x8*)(lds + ((ks & 1) ? d1 : d0) + (ks >> 1) * 8192 + n * 2048);
      };
      rdd(0); rdd(1);
#pragma unroll
      for (int h = 0; h < 2; ++h)
#pragma unroll
        for (int n = 0; n < 4; ++n) ac[h][n] = (f32x4){0, 0, 0, 0};
      SCHED();
#pragma unroll
      for (int ks = 0; ks < 14; ++ks) {
        if (ks + 2 < 14) rdd(ks + 2);
#pragma unroll
        for (int n = 0; n < 4; ++n)
#pragma unroll
          for (int h = 0; h < 2; ++h) ac[h][n] = MFMA(xf[ks][h], df[ks][n], ac[h][n]);
        if (ks + 2 < 14) {
#pragma unroll
          for (int i = 0; i < 4; ++i) { SGB(0x8, 2); SGB(0x100, 1); }
        }
        SCHED();
      }
      {
#pragma unroll
        for (int n = 0; n < 4; ++n) {
          const u32x4 o = {pk_bf16(ac[0][n][0], ac[0][n][1]), pk_bf16(ac[0][n][2], ac[0][n][3]),
                           pk_bf16(ac[1][n][0], ac[1][n][1]), pk_bf16(ac[1][n][2], ac[1][n][3])};
          const int T = 4 * zg + n, pp = T / 7, n7 = T % 7;
          st128(rws, o, lane * 16 + (OFF_BT + ((pp * 64 + kb) * 7 + n7) * 1024));
        }
      }
    }
    SCHED();
    STAMP(5);
    BAR();
    STAMP(6);
    bf16x8 wf[14][4];
    auto rdw = [&](int ks) {
#pragma unroll
      for (int n = 0; n < 4; ++n) {
        LAS char* a = lds + (bw[n] - (R1 + 16384 - W1C0)) + (ks < 2 ? 0 : W1 - W1C0) + ks * 4096;
        wf[ks][n] = CAT(TRD(a), TRD(a + 512));
      }
    };
    {
      f32x2 st[2];
#pragma unroll
      for (int h = 0; h < 2; ++h) st[h] = *(const LAS f32x2*)(lds + STAT + wid * 256 + (h * 16 + fi) * 8);
      rdw(0); rdw(1);
      LAS char* sc = lds + SCR + wid * 4096;
#pragma unroll
      for (int h = 0; h < 2; ++h)
#pragma unroll
        for (int n = 0; n < 4; ++n) {
          const f32x4 v = z2[h][n] * st[h].x + st[h].y;
          const u32x2 o = {pk_bf16(v[0], v[1]), pk_bf16(v[2], v[3])};
          *(LAS u32x2*)(sc + (arow + 4 * h) * 128 + (((4 * n + fq) ^ fi) << 3)) = o;
        }
      WAITL0();
      {
#pragma unroll
        for (int n = 0; n < 4; ++n) {
          const unsigned a = (8 * fq + tq) * 128 + (((4 * n + tp) ^ ((fq << 2) | tq)) << 3);
          const bf16x8 o = CAT(TRD(sc + a), TRD(sc + a + 512));
          const int T = 56 + 4 * zg + n, pp = T / 7, n7 = T % 7;
          st128(rws, *(const u32x4*)&o, lane * 16 + (OFF_BT + ((pp * 64 + kb) * 7 + n7) * 1024));
        }
      }
    }
#pragma unroll
    for (int h = 0; h < 2; ++h)
#pragma unroll
      for (int n = 0; n < 4; ++n) ac[h][n] = (f32x4){0, 0, 0, 0};
    SCHED();
    STAMP(7);
#pragma unroll
    for (int ks = 0; ks < 14; ++ks) {
      if (ks + 2 < 14) rdw(ks + 2);
#pragma unroll
      for (int n = 0; n < 4; ++n)
#pragma unroll
        for (int h = 0; h < 2; ++h) ac[h][n] = MFMA(wf[ks][n], xf[ks][h], ac[h][n]);
      if (ks + 2 < 14) {
#pragma unroll
        for (int i = 0; i < 8; ++i) { SGB(0x8, 1); SGB(0x100, 1); }
      }
      SCHED();
      if (ks == FLAGKS) { arrive_and_flag(lds, 0, ws, OFF_F1 + (zg * 16 + mi) * 128, tag, lane); SCHED(); STAMP(8); }
    }
#pragma unroll
    for (int h = 0; h < 2; ++h)
#pragma unroll
      for (int k = 0; k < 2; ++k) {
        const f32x4 v = gelu4(ac[h][2 * k]), w = gelu4(ac[h][2 * k + 1]);
        z1o[h][k] = (u32x4){pk_bf16(v[0], v[1]), pk_bf16(v[2], v[3]), pk_bf16(w[0], w[1]), pk_bf16(w[2], w[3])};
      }
  }
  f32x4 acc[2][7];
#pragma unroll
  for (int m = 0; m < 2; ++m) {
    const float bv = G ? bflo(braw[m]) : 0.f;
#pragma unroll
    for (int n = 0; n < 7; ++n) { acc[m][n] = (f32x4){bv, bv, bv, bv}; asm volatile("" : "+v"(acc[m][n])); }
  }
  u32x4 zq[2][3];
  u32x2 zs[2];
  {
    const unsigned boff = STA + lane * 16;
    bf16x8 fa[2][2][2], fb[2][2][7];
    unsigned ba[2][2] = {{aoff0, aoff1}, {aoff0 + 2 * STG, aoff1 + 2 * STG}}, bb[2] = {boff, boff + 2 * STG};
    asm volatile("" : "+v"(ba[1][0]), "+v"(ba[1][1]), "+v"(bb[1]));
    auto rd = [&](int set, int slot) {
      const int so = (slot & 1) * STG;
#pragma unroll
      for (int k = 0; k < 2; ++k) {
#pragma unroll
        for (int m = 0; m < 2; ++m) fa[set][k][m] = *(const LAS bf16x8*)(lds + ba[slot >> 1][k] + (so + m * 512));
#pragma unroll
        for (int n = 0; n < 7; ++n) fb[set][k][n] = *(const LAS bf16x8*)(lds + bb[slot >> 1] + (so + k * 7168 + n * 1024));
      }
    };
    auto mm = [&](int set) {
#pragma unroll
      for (int k = 0; k < 2; ++k)
#pragma unroll
        for (int n = 0; n < 7; ++n)
#pragma unroll
          for (int m = 0; m < 2; ++m) acc[m][n] = MFMA(fb[set][k][n], fa[set][k][m], acc[m][n]);
    };
    auto sched = [&]() {
#pragma unroll
      for (int i = 0; i < 18; ++i) { SGB(0x8, 1); SGB(0x100, 1); }
      SGB(0x8, 10);
      SCHED();
    };
    STAMP(9);
    WAITL0();
    BAR();
    STAMP(10);
    CSTAMP(17);
    rd(0, 0);
    if (!spare) {
#pragma unroll
      for (int j = 0; j < 4; ++j) st128(rws, z1o[j >> 1][j & 1], lane * 16 + OFF_Z1 + ((2 * kb + (j >> 1)) * 28 + 2 * zg + (j & 1)) * 1024);
    }
    SCHED();
#pragma unroll 1
    for (int t = 0; t < 7; ++t) {
      WAITL0(); BAR(); rd(1, 1); mm(0); sched();
      WAITL0(); BAR(); rd(0, 2); mm(1); sched();
      WAITL0(); BAR(); rd(1, 3); mm(0); sched();
      WAITL0(); BAR(); rd(0, 0); mm(1); sched();
      if (__builtin_expect(t == 0, 0)) arrive_and_flag(lds, 1, ws, OFF_F3 + (mi * 16 + zg) * 128, tag, lane);
    }
    WAITL0(); BAR(); rd(1, 1); mm(0); sched();
    WAITL0(); BAR(); rd(0, 2); mm(1); sched();
    WAITL0(); BAR(); rd(1, 3); mm(0); sched();
    WAITL0(); BAR();
    {
      const int odd = p & 1;
      LAS char* zl = lds + SPARE + wid * 8192 + lane * 16;
#pragma unroll
      for (int m = 0; m < 2; ++m) {
#pragma unroll
        for (int k = 0; k < 3; ++k) zq[m][k] = *(const LAS u32x4*)(zl + odd * 1024 + (m * 4 + k) * 1024);
        zs[m] = *(const LAS u32x2*)(zl + (odd ? 8 : 3072) + m * 4096);
      }
    }
    mm(1);
    SCHED();
  }
  STAMP(11);
  CSTAMP(18);
  {
#pragma unroll
    for (int m = 0; m < 2; ++m) asm volatile("" : "+v"(zq[m][0]), "+v"(zq[m][1]), "+v"(zq[m][2]), "+v"(zs[m]));
    const unsigned o2 = OFF_O2 + (2 * kb * 56 + ((7 * p) >> 1)) * 1024;
    auto epi = [&](auto OC, auto GC) {
      constexpr int ODD = decltype(OC)::value;
      constexpr bool GG = decltype(GC)::value;
      auto fin = [&](f32x4 v, unsigned z0, unsigned z1) {
        if (GG) v *= (f32x4){bflo(z0), bfhi(z0), bflo(z1), bfhi(z1)};
        return (u32x2){pk_bf16(v[0], v[1]), pk_bf16(v[2], v[3])};
      };
#pragma unroll
      for (int m = 0; m < 2; ++m) {
        const unsigned om = o2 + m * 56 * 1024;
#pragma unroll
        for (int k = 0; k < 3; ++k) {
          const u32x2 lo = fin(acc[m][2 * k + ODD], zq[m][k][0], zq[m][k][1]), hi = fin(acc[m][2 * k + 1 + ODD], zq[m][k][2], zq[m][k][3]);
          st128(rws, (u32x4){lo[0], lo[1], hi[0], hi[1]}, lane * 16 + (om + (k + ODD) * 1024));
        }
        __builtin_amdgcn_raw_buffer_store_b64(fin(acc[m][ODD ? 0 : 6], zs[m][0], zs[m][1]), rws, lane * 16, om + (ODD ? 8 : 3072), 16);
      }
    };
    if (G) { if (p & 1) epi(std::integral_constant<int, 1>{}, std::true_type{}); else epi(std::integral_constant<int, 0>{}, std::true_type{}); }
    else { if (p & 1) epi(std::integral_constant<int, 1>{}, std::false_type{}); else epi(std::integral_constant<int, 0>{}, std::false_type{}); }
  }
  STAMP(12);
  arrive_and_flag(lds, 2, ws, OFF_F2 + (mi * 16 + p) * 128, tag, lane);
  STAMP(13);
  BAR();
  if (r >= 28) { STAMP_FLUSH(); return; }
  const int mt = 4 * rg + wid;
  unsigned wb[4];
#pragma unroll
  for (int n = 0; n < 4; ++n) wb[n] = (4 * fq + tq) * 128 + ((n ^ (((4 * fq + tq) >> 1) & 3)) << 5) + tp * 8;
  bf16x4 wl[2][16], wh[2][16];
  auto rdw = [&](int c) {
#pragma unroll
    for (int i = 0; i < 16; ++i) {
      LAS char* a = lds + wb[i & 3] + (wo_loc(c) + (i >> 2) * 4096);
      wl[c & 1][i] = TRD(a);
      wh[c & 1][i] = TRD(a + 2048);
    }
  };
  rdw(0);
  SCHED();
  BAR();
  STAMP(14);
  const unsigned o2 = OFF_O2 + mt * 56 * 1024;
  u32x4 hf[7][4];
  auto ldh = [&](int c) {
    int cc = c + rot3;
    if (cc >= 7) cc -= 7;
#pragma unroll
    for (int k = 0; k < 4; ++k) hf[c][k] = __builtin_amdgcn_raw_buffer_load_b128(rws, lane * 16 + k * 1024, o2 + (28 + cc * 4) * 1024, 16);
  };
  ldh(0); ldh(1); ldh(2); ldh(3);
  SCHED();
  u32x4 yr[2], yi[2];
  float g;
  const int t = 32 * (mt >> 1) + arow + 4 * (mt & 1);
  f32x4 oc[4];
#pragma unroll
  for (int n = 0; n < 4; ++n) oc[n] = (f32x4){0, 0, 0, 0};
#pragma unroll
  for (int c = 0; c < 7; ++c) {
    if (c < 3) ldh(c + 4);
    if (c == 3) {
#pragma unroll
      for (int k = 0; k < 2; ++k) {
        yr[k] = __builtin_amdgcn_raw_buffer_load_b128(rws, lane * 16 + k * 1024, o2 + 2 * dg * 1024, 16);
        yi[k] = __builtin_amdgcn_raw_buffer_load_b128(rws, lane * 16 + k * 1024, o2 + (14 + 2 * dg) * 1024, 16);
      }
      g = __hip_atomic_load((GAS float*)(ws + OFF_G) + t, RLX_AGENT);
    }
    SCHED();
    if (c < 6) rdw(c + 1);
#pragma unroll
    for (int i = 0; i < 16; ++i) oc[i & 3] = MFMA(CAT(wl[c & 1][i], wh[c & 1][i]), *(const bf16x8*)&hf[c][i >> 2], oc[i & 3]);
    if (c < 6) {
#pragma unroll
      for (int i = 0; i < 16; ++i) { SGB(0x8, 1); SGB(0x100, 2); }
    }
    SCHED();
  }
  STAMP(15);
  const float g1 = 1.0f - g;
#pragma unroll
  for (int n = 0; n < 4; ++n) {
    const unsigned r0 = yr[n >> 1][(n & 1) * 2], r1 = yr[n >> 1][(n & 1) * 2 + 1];
    const unsigned i0 = yi[n >> 1][(n & 1) * 2], i1 = yi[n >> 1][(n & 1) * 2 + 1];
    const f32x4 y = {bflo(r0) - bflo(i0), bfhi(r0) - bfhi(i0), bflo(r1) - bflo(i1), bfhi(r1) - bfhi(i1)};
    const f32x4 v = y * g + oc[n] * g1;
    *(u32x2*)(out + (size_t)t * D + 64 * dg + 16 * n + 4 * fq) = (u32x2){pk_bf16(v[0], v[1]), pk_bf16(v[2], v[3])};
  }
  STAMP(16);
  STAMP_FLUSH();
}

extern "C" void kernel_launch(void* const* d_in, const int* in_sizes, int n_in, void* d_out, int out_size, void* d_ws,
                              size_t ws_size, hipStream_t stream) {
  fused<<<256, 512, 0, stream>>>((const u16*)d_in[0], (const u16*)d_in[1], (const u16*)d_in[2], (const u16*)d_in[3],
                                 (const u16*)d_in[4], (const u16*)d_in[6], (const u16*)d_in[7], (const u16*)d_in[8],
                                 (const u16*)d_in[9], (const u16*)d_in[10], (char*)d_ws, (u16*)d_out);
}
```

```cpp
#include <hip/hip_runtime.h>
#include <hip/hip_bf16.h>
#include <type_traits>

typedef unsigned short u16;
typedef unsigned long long u64;
typedef short bf16x8 __attribute__((ext_vector_type(8)));
typedef short bf16x4 __attribute__((ext_vector_type(4)));
typedef float f32x2 __attribute__((ext_vector_type(2)));
typedef float f32x4 __attribute__((ext_vector_type(4)));
typedef unsigned u32x2 __attribute__((ext_vector_type(2)));
typedef unsigned u32x4 __attribute__((ext_vector_type(4)));
#define LAS __attribute__((address_space(3)))
#define GAS __attribute__((address_space(1)))
#define RLX_AGENT __ATOMIC_RELAXED, __HIP_MEMORY_SCOPE_AGENT

#define WAITV(n) asm volatile("s_waitcnt vmcnt(%0)" ::"n"(n) : "memory")
#define WAITL0() do { asm volatile("" ::: "memory"); __builtin_amdgcn_s_waitcnt(0xC07F); asm volatile("" ::: "memory"); } while (0)
#define BAR() do { asm volatile("" ::: "memory"); __builtin_amdgcn_s_barrier(); asm volatile("" ::: "memory"); } while (0)
#define SCHED() __builtin_amdgcn_sched_barrier(0)
#define SGB(mask, n) __builtin_amdgcn_sched_group_barrier(mask, n, 0)
#define MFMA(a, b, c) __builtin_amdgcn_mfma_f32_16x16x32_bf16(a, b, c, 0, 0, 0)
#define TRD(p) __builtin_amdgcn_ds_read_tr16_b64_v4i16((LAS bf16x4*)(p))
#define CAT(lo, hi) __builtin_shufflevector(lo, hi, 0, 1, 2, 3, 4, 5, 6, 7)

template <bool SC1 = false>
__device__ __forceinline__ void glds(const void* sbase, unsigned voff, unsigned l) {
  l = __builtin_amdgcn_readfirstlane(l);
  if (SC1) asm volatile("s_mov_b32 m0, %2\n\ts_nop 0\n\tglobal_load_lds_dwordx4 %0, %1 sc1" :: "v"(voff), "s"(sbase), "s"(l) : "memory");
  else asm volatile("s_mov_b32 m0, %2\n\ts_nop 0\n\tglobal_load_lds_dwordx4 %0, %1" :: "v"(voff), "s"(sbase), "s"(l) : "memory");
}
__device__ __forceinline__ void glds_a8(const void* g, const unsigned (&o)[8], unsigned l0, unsigned l1) {
  asm volatile("s_mov_b32 m0, %9\n\ts_nop 0\n\t"
               "global_load_lds_dwordx4 %0, %8\n\tglobal_load_lds_dwordx4 %1, %8 offset:1024\n\t"
               "global_load_lds_dwordx4 %2, %8 offset:2048\n\tglobal_load_lds_dwordx4 %3, %8 offset:3072\n\t"
               "s_mov_b32 m0, %10\n\ts_nop 0\n\t"
               "global_load_lds_dwordx4 %4, %8\n\tglobal_load_lds_dwordx4 %5, %8 offset:1024\n\t"
               "global_load_lds_dwordx4 %6, %8 offset:2048\n\tglobal_load_lds_dwordx4 %7, %8 offset:3072"
               :: "v"(o[0]), "v"(o[1]), "v"(o[2]), "v"(o[3]), "v"(o[4]), "v"(o[5]), "v"(o[6]), "v"(o[7]), "s"(g), "s"(l0), "s"(l1) : "memory");
}
__device__ __forceinline__ void glds_b7(const void* g, const unsigned (&o)[8], unsigned l0, unsigned l1) {
  asm volatile("s_mov_b32 m0, %3\n\ts_nop 0\n\t"
               "global_load_lds_dwordx4 %0, %2 sc1\n\tglobal_load_lds_dwordx4 %0, %2 offset:1024 sc1\n\t"
               "global_load_lds_dwordx4 %0, %2 offset:2048 sc1\n\tglobal_load_lds_dwordx4 %0, %2 offset:3072 sc1\n\t"
               "s_mov_b32 m0, %4\n\ts_nop 0\n\t"
               "global_load_lds_dwordx4 %1, %2 sc1\n\tglobal_load_lds_dwordx4 %1, %2 offset:1024 sc1\n\t"
               "global_load_lds_dwordx4 %1, %2 offset:2048 sc1"
               :: "v"(o[0]), "v"(o[4]), "s"(g), "s"(l0), "s"(l1) : "memory");
}
__device__ __forceinline__ void touch(const void* sbase, unsigned voff, unsigned l) {
  l = __builtin_amdgcn_readfirstlane(l);
  asm volatile("s_mov_b32 m0, %2\n\ts_nop 0\n\tglobal_load_lds_dword %0, %1" :: "v"(voff), "s"(sbase), "s"(l) : "memory");
}
#define PWA 3
#define PWB 4
#define PWBS 12
#define NOZ1 0
#define PWW 2
#define PWR 0
#define NSG 0
#define YAT 12
#define FARLEAD 1
#define LPRIO 0
#define WTS 0
#define PROG 1
#define AHOLD 0
#define ASC1 0
#define W16 1
#define INITG 1
#define YSPLIT 1
#define HTOUCH 0
#define HROT 1
#define HAH 5
#define PHOLD 1
#define PSLEEP 0
#define PRE1 3
#define NSP 4
#define GSLEEP 2
#define ASLEEP 0
#define BSLEEP 10
#define FLAGKS 6
#define CODE_KB 28
#define BSC1 true
constexpr int S = 2048, D = 448;

__device__ __forceinline__ float bf2f(u16 v) { return __uint_as_float(((unsigned)v) << 16); }
__device__ __forceinline__ float bflo(unsigned v) { return __uint_as_float(v << 16); }
__device__ __forceinline__ float bfhi(unsigned v) { return __uint_as_float(v & 0xffff0000u); }
__device__ __forceinline__ unsigned pk_bf16(float lo, float hi) {
  typedef __bf16 bf2 __attribute__((ext_vector_type(2)));
  f32x2 v = {lo, hi};
  bf2 b = __builtin_convertvector(v, bf2);
  return *(unsigned*)&b;
}
__device__ __forceinline__ f32x2 gelu2(f32x2 v) {
  const float c0 = -2.0f * 0.7978845608028654f * 1.4426950408889634f;
  const float c1 = c0 * 0.044715f;
  const f32x2 u = v * ((v * v) * c1 + c0);
  f32x2 e = {__builtin_amdgcn_exp2f(u.x), __builtin_amdgcn_exp2f(u.y)};
  e = e + 1.0f;
  const f32x2 rc = {__builtin_amdgcn_rcpf(e.x), __builtin_amdgcn_rcpf(e.y)};
  return v * rc;
}
__device__ __forceinline__ f32x4 gelu4(f32x4 v) {
  const f32x2 lo = gelu2((f32x2){v[0], v[1]}), hi = gelu2((f32x2){v[2], v[3]});
  return (f32x4){lo.x, lo.y, hi.x, hi.y};
}
__device__ __forceinline__ float xsum(float s) {
  auto a = __builtin_amdgcn_permlane16_swap(__float_as_uint(s), __float_as_uint(s), false, false);
  s = __uint_as_float(a[0]) + __uint_as_float(a[1]);
  auto b = __builtin_amdgcn_permlane32_swap(__float_as_uint(s), __float_as_uint(s), false, false);
  return __uint_as_float(b[0]) + __uint_as_float(b[1]);
}

constexpr unsigned OFF_BT = 0;
constexpr unsigned OFF_O2 = OFF_BT + 1792 * S * 2;
constexpr unsigned OFF_O1 = OFF_O2 + 128 * 48 * 1024;
constexpr unsigned OFF_Z1 = OFF_O2 + 1792 * S * 2;
constexpr unsigned OFF_G = OFF_Z1 + 896 * S * 2;
constexpr unsigned OFF_X = OFF_G + 8192;
constexpr unsigned OFF_F1 = OFF_X + 64 * 14 * 512;
constexpr unsigned OFF_F2 = OFF_F1 + 256 * 128;
constexpr unsigned OFF_F3 = OFF_F2 + 256 * 128;
constexpr unsigned OFF_E = OFF_F3 + 256 * 128;
constexpr unsigned WS_USED = OFF_E + 128;
constexpr u64 EPOCH_STEP = 0x9E3779B97F4A7C15ull;
constexpr unsigned SPIN_CAP = 1u << 20;

#define CSTAMP(i)
#define STAMP_DECL
#define STAMP(i)
#define STAMP_FLUSH()

constexpr unsigned R1 = 89600, R1S = 24576, SCR = 89600, W1 = 105984;
constexpr int STA = 16384, STB = 14336, STG = STA + STB, RING = 4, NT = 32, LEAD = 4;
constexpr unsigned SPARE = RING * STG;
constexpr unsigned W1C0 = 77824;
constexpr unsigned STAT = 86016, FLG = 155648, CNT = 163328, JUNK = 163584;
__device__ __forceinline__ unsigned bt_pos(int T, int kb) {
  const int pp = T / 7, n7 = T % 7, j = (pp & 1) ? (n7 + 6) % 7 : n7;
  return OFF_BT + ((pp * 64 + kb) * 7 + j) * 1024;
}

__device__ __forceinline__ void st128(__amdgpu_buffer_rsrc_t rs, u32x4 v, unsigned off) {
  __builtin_amdgcn_raw_buffer_store_b128(v, rs, off, 0, 16);
}
__device__ __forceinline__ void wait_flags(GAS u64* p, u64 tag) {
#pragma unroll 1
  for (unsigned spins = 0; spins < SPIN_CAP; ++spins) {
    const u64 v = __hip_atomic_load(p, RLX_AGENT);
    if (__all(v == tag)) break;
    __builtin_amdgcn_s_sleep(2);
  }
  __builtin_amdgcn_fence(__ATOMIC_ACQUIRE, "wavefront");
}

__device__ __forceinline__ void arrive_and_flag(LAS char* lds, int which, char* ws, unsigned flag_off, u64 tag, int lane) {
  WAITV(0);
  if (lane == 0) {
    const unsigned old = __hip_atomic_fetch_add((LAS unsigned*)(lds + CNT) + which, 1u, __ATOMIC_RELAXED, __HIP_MEMORY_SCOPE_WORKGROUP);
    if (old == 3) __hip_atomic_store((GAS u64*)(ws + flag_off), tag, RLX_AGENT);
  }
}

__global__ __launch_bounds__(512, 2) void fused(const u16* __restrict__ x, const u16* __restrict__ wsr, const u16* __restrict__ wsi,
                                                const u16* __restrict__ wdr, const u16* __restrict__ wdi, const u16* __restrict__ win,
                                                const u16* __restrict__ wsp, const u16* __restrict__ bsp, const u16* __restrict__ wout,
                                                const u16* __restrict__ gw, char* __restrict__ ws, u16* __restrict__ out) {
  __shared__ __attribute__((aligned(1024))) char smem[163840];
  LAS char* lds = (LAS char*)smem;
  const int wid = __builtin_amdgcn_readfirstlane(threadIdx.x >> 6), lane = threadIdx.x & 63;
  STAMP_DECL; STAMP(0);
  const int b = blockIdx.x, xcd = b & 7, r = b >> 3;
  const bool G = xcd < 4;
  const int ib = G ? (r & 3) : (r & 7), ia = G ? (r >> 2) : (r >> 3);
  const int mi = (G ? xcd * 4 : (xcd & 1) * 8) + ib;
  const int p = (G ? 8 : (xcd & 2) * 2) + ia;
  const int cls = (ia + ib) & 3;
  const bool spare = (p & 11) == 3;
  const int zg = (int)((0xdb976420fca8e531ull >> (4 * p)) & 15);
  const unsigned o7 = (cls & 2) ? ((cls & 1) ? 0x6423501u : 0x6542031u) : ((cls & 1) ? 0x6532410u : 0x5463120u);
  const unsigned ldsb = __builtin_amdgcn_readfirstlane((unsigned)(size_t)lds);
  if (!spare) {
    const unsigned l3 = lane >> 3, l7 = lane & 7, l4 = (lane >> 4) & 1;
    const char* xg = (const char*)x + (size_t)(mi * 128 + wid * 16) * 896;
    const unsigned xo0 = l3 * 896 + ((l7 ^ (l4 | (((2 * wid) & 3) << 1))) << 4);
    const unsigned xo1 = (xo0 ^ 32) + 8 * 896;
    const char* wg = (const char*)win + (size_t)(wid * 8) * 3584 + 1792 + zg * 128;
    const unsigned wo = l3 * 3584 + (((((l7 >> 1) ^ (l4 | ((wid & 1) << 1))) << 1) | (l7 & 1)) << 4);
#pragma unroll
    for (int s = 0; s < 2; ++s) {
      const unsigned st = (o7 >> (4 * s)) & 7;
      glds(xg + st * 128, xo0, ldsb + R1 + s * R1S + wid * 2048);
      glds(xg + st * 128, xo1, ldsb + R1 + s * R1S + wid * 2048 + 1024);
      glds(wg + st * (64 * 3584), wo, ldsb + R1 + s * R1S + 16384 + wid * 1024);
    }
  }
  if (wid == 7 && CODE_KB > 0) {
    u64 pc;
    asm volatile("s_getpc_b64 %0" : "=s"(pc));
    const unsigned o = (r * 8 + (lane & 7)) * 128;
    touch((const char*)(pc & ~127ull), o < CODE_KB * 1024 ? o : 0, ldsb + JUNK);
  }
  u64 tag = *(const __attribute__((address_space(4))) u64*)(ws + OFF_E);
  const int rg = 4 * xcd + (r & 3), dg = r >> 2, rot3 = HROT ? (dg + r) & 3 : r & 3;

  if (wid >= 4) {
    const int lw = wid - 4;
    if (LPRIO) __builtin_amdgcn_s_setprio(LPRIO);
    int lane = threadIdx.x;
    asm volatile("" : "+v"(lane));
    lane &= 63;
    const unsigned l3 = lane >> 3, l7 = lane & 7, l4 = (lane >> 4) & 1;
    const u16* am = p < 4 ? wsr : wsi;
    am = G ? wsp : am;
    asm volatile("" : "+s"(tag));
    tag += EPOCH_STEP;
    const unsigned pwo = (lane >> 2) * 4096 + (lane & 3) * 128;
    if (spare) {
      BAR();
      touch(wout, (unsigned)(((mi * 2 + (p >> 2)) * 4 + lw) * 49 + (lane < 49 ? lane : 48)) * 128, ldsb + JUNK);
      const char* pw = (const char*)am + (size_t)(mi * 128 + lw * 32) * 4096;
#pragma unroll 1
      for (int j = 0; j < NSP; ++j) {
        touch(pw + j * 512, pwo, ldsb + JUNK);
        touch(pw + j * 512 + 16 * 4096, pwo, ldsb + JUNK);
      }
      const char* ps = (const char*)wsp + (size_t)((mi * 2 + (p >> 2)) * 64 + lw * 16) * 4096 + PWA * 512;
#pragma unroll 1
      for (int j = 0; j < NSG; ++j) touch(ps + j * 512, pwo, ldsb + JUNK);
      WAITV(0);
      STAMP(6);
    } else {
      const char* xg = (const char*)x + (size_t)(mi * 128 + lw * 32) * 896;
      const unsigned xo = l3 * 896 + ((l7 ^ l4) << 4);
      const char* wg = (const char*)win + (size_t)(lw * 16) * 3584 + zg * 128;
      const unsigned wo = l3 * 3584 + (((((l7 >> 1) ^ l4) << 1) | (l7 & 1)) << 4);
      const u16* dm = zg < 7 ? wdr : wdi;
      STAMP(1);
      WAITV(3);
      BAR();
      int slot = 2;
#pragma unroll 1
      for (int s = 2; s < 7; ++s) {
        const unsigned st = (o7 >> (4 * s)) & 7, l = ldsb + R1 + slot * R1S;
#pragma unroll
        for (int i = 0; i < 4; ++i) glds(xg + st * 128 + i * (8 * 896), xo ^ (i << 5), l + lw * 4096 + i * 1024);
#pragma unroll
        for (int i = 0; i < 2; ++i) glds(wg + st * (64 * 3584) + i * (8 * 3584) + 1792, wo ^ (i << 6), l + 16384 + lw * 2048 + i * 1024);
        slot = slot == 2 ? 0 : slot + 1;
        if (s == 6 && WTS) {
          const unsigned nl = G ? 56 : 28, u = (2 * ib + (lw & 1)) * nl + min((unsigned)lane, nl - 1);
          if (lw < 2) touch((const char*)dm + (size_t)((zg < 7 ? zg : zg - 7) * 64) * 896, u * 128, ldsb + JUNK);
          else touch((const char*)win + zg * 128, u * 3584, ldsb + JUNK);
          WAITV(7);
        } else {
          WAITV(6);
        }
        BAR();
      }
      const char* dg_ = (const char*)dm + (size_t)((zg < 7 ? zg : zg - 7) * 64 + lw * 16) * 896;
      const unsigned do_ = l3 * 896 + ((l7 ^ (l3 >> 1)) << 4);
      auto dchunk = [&](int c) {
        const unsigned st = (o7 >> (4 * c)) & 7;
#pragma unroll
        for (int i = 0; i < 2; ++i) glds(dg_ + st * 128 + i * (8 * 896), do_ ^ (i << 6), ldsb + c * 8192 + lw * 2048 + i * 1024);
      };
      auto wchunk = [&](int c) {
        const unsigned st = (o7 >> (4 * c)) & 7;
#pragma unroll
        for (int i = 0; i < 2; ++i)
          glds(wg + st * (64 * 3584) + i * (8 * 3584), wo ^ (i << 6), ldsb + (c ? W1 + c * 8192 : W1C0) + lw * 2048 + i * 1024);
      };
      dchunk(0); dchunk(1); dchunk(2);
      if (WTS) WAITV(7); else WAITV(6);
      BAR();
#pragma unroll 1
      for (int c = 3; c < 7; ++c) dchunk(c);
#pragma unroll 1
      for (int c = 0; c < 7; ++c) wchunk(c);
      const char* pw = (const char*)am + (size_t)(mi * 128) * 4096;
      auto warm = [&](int j) {
        if (G) { if (j < 8) touch(pw + (size_t)ia * 16 * 4096 + j * 512, pwo, ldsb + JUNK); }
        else { const int rw = ia * 3 + j % 3, bl = NSP + j / 3; if (rw < 8 && bl < 8) touch(pw + (size_t)rw * 16 * 4096 + bl * 512, pwo, ldsb + JUNK); }
      };
      WAITV(14);
      STAMP(2);
      BAR();
      if (G && lw < PWA) warm(lw);
    }
    const bool isA = lw < 2;
    unsigned off[8];
#pragma unroll
    for (int i = 0; i < 8; ++i) {
      off[i] = (isA ? (8 * i + l3) * 4096 + ((l7 ^ (l4 | ((i & 3) << 1))) << 4) : i * 1024 + lane * 16) - (i & 3) * 1024;
      asm volatile("" : "+v"(off[i]));
    }
    const char* base2 = isA ? (const char*)am + (size_t)(mi * 128 + lw * 64) * 4096 : ws + OFF_BT + (size_t)p * 64 * 7 * 1024 + (lw - 2) * 7168;
    const unsigned step2 = isA ? 128 : STB, l0 = ldsb + (isA ? lw * 8192 : STA + (lw - 2) * 7168);
    const int pj = p & 7, g0 = (7 * pj) >> 2, g1 = (7 * pj + 6) >> 2;
    const int g2 = g1 - g0 == 2 ? g0 + 1 : (pj & 1 ? 14 + (pj >> 2) : g0);
    unsigned f1o = OFF_F1 + ((lane < 16 ? g0 : (lane < 32 ? g1 : g2)) * 16 + (lane & 15)) * 128;
    unsigned f3o = OFF_F3 + (mi * 16 + ((lane & 2) ? (g1 == zg ? g0 : g1) : (g0 == zg ? g1 : g0))) * 128;
    unsigned wo_off = l3 * 896 + (((((l7 >> 1) ^ ((lane >> 4) & 3)) << 1) | (l7 & 1)) << 4);
    asm volatile("" : "+v"(f1o), "+v"(f3o), "+v"(wo_off));
    const int qflag = (G && lw == 0) ? 1 : -1, qz = G ? 2 + (INITG == 2 ? (ib >> 1) : (ib * 3) >> 2) : -1;
    SCHED();
    if (!spare) {
      __builtin_amdgcn_s_sleep(GSLEEP);
      const __amdgpu_buffer_rsrc_t rx = __builtin_amdgcn_make_buffer_rsrc(ws, 0, WS_USED, 0x00020000);
      const u64 tag40 = tag << 24;
      u32x4 gr[7];
      STAMP(8);
#pragma unroll 1
      for (unsigned spins = 0; spins < SPIN_CAP; ++spins) {
        unsigned bh = 0, bl = 0;
        asm volatile("" ::: "memory");
#pragma unroll
        for (int c = 0; c < 7; ++c)
          gr[c] = __builtin_amdgcn_raw_buffer_load_b128(rx, lane * 16 + (c & 3) * 1024, OFF_X + (4 * mi + lw) * 7168 + (c >> 2) * 4096, 16);
#pragma unroll
        for (int c = 0; c < 7; ++c) {
          bh |= (gr[c][1] ^ (unsigned)(tag40 >> 32)) | (gr[c][3] ^ (unsigned)(tag40 >> 32));
          bl |= (gr[c][0] ^ (unsigned)tag40) | (gr[c][2] ^ (unsigned)tag40);
        }
        if (__all((bh | (bl >> 24)) == 0)) break;
      }
      STAMP(12);
      auto both = [](float v) {
        auto q = __builtin_amdgcn_permlane32_swap(__float_as_uint(v), __float_as_uint(v), false, false);
        return __uint_as_float(q[0]) + __uint_as_float(q[1]);
      };
      float sum = 0.f, m2 = 0.f, dev = 0.f;
#pragma unroll
      for (int c = 0; c < 7; ++c) { sum += __uint_as_float(gr[c][0] << 8); m2 += __uint_as_float(gr[c][2] << 8); }
      const float mean = both(sum) * (1.0f / 14.0f);
#pragma unroll
      for (int c = 0; c < 7; ++c) { const float d = __uint_as_float(gr[c][0] << 8) - mean; dev = __builtin_fmaf(d, d, dev); }
      const float rstd = __builtin_amdgcn_rsqf(both(__builtin_fmaf(dev, 64.0f, m2)) * (1.0f / 896.0f) + 1e-5f);
      *(LAS float*)(lds + STAT + lw * 256 + (lane >> 5) * 128 + (8 * ((lane & 15) >> 2) + 4 * ((lane >> 4) & 1) + (lane & 3)) * 4) =
          lane < 32 ? rstd : -mean * rstd;
      WAITV(0);
      WAITL0();
      STAMP(6);
      BAR();
    }
    const int ini = G ? INITG : 1, hx = (ini == 2) ? (ia >> 2) : 0;
    const int np = ini + cls + (ini + cls < 3 ? 5 : 1), nl = ini + cls + 1 + 4 * (7 - ini);
    unsigned ready = 0;
    int n = 0;
#pragma unroll 1
    for (; n < np; ++n) {
      const int m = n - ini;
      const int st = m < 0 ? cls + 4 * n : ((m & 3) == cls ? cls + 4 * (((m >> 2) + ini) ^ hx) : m);
      const char* g = base2 + (size_t)st * step2;
      unsigned l = l0 + (n & (RING - 1)) * STG;
      asm volatile("" : "+s"(g), "+s"(l));
      if (n >= 4) {
        WAITV(14);
        BAR();
      } else if (n == 2) {
        WAITV(7);
        STAMP(3);
        BAR();
      }
      if (ready != 0xffffu) {
        if (n == 0) {
#pragma unroll 1
          for (unsigned spins = 0; spins < SPIN_CAP && !spare; ++spins) {
            if (__hip_atomic_load((LAS unsigned*)(lds + CNT), __ATOMIC_RELAXED, __HIP_MEMORY_SCOPE_WORKGROUP) == 4) break;
            __builtin_amdgcn_s_sleep(1);
          }
          STAMP(7);
          if (isA) ready = 0xffffu; else __builtin_amdgcn_s_sleep(BSLEEP);
        }
        const unsigned need = (PROG && n < np - 1) ? 1u << (st >> 1) : 0xffffu;
#pragma unroll 1
        for (unsigned spins = 0; (ready & need) != need && spins < SPIN_CAP; ++spins) {
          if (spins) __builtin_amdgcn_s_sleep(1);
          const u64 m2 = __ballot(__hip_atomic_load((GAS u64*)(ws + f1o), RLX_AGENT) == tag);
          ready = (unsigned)(m2 & (m2 >> 16) & (m2 >> 32)) & 0xffffu;
        }
        __builtin_amdgcn_fence(__ATOMIC_ACQUIRE, "wavefront");
        if (n == 0) STAMP(4);
      }
      if (isA) glds_a8(g, off, l, l + 4096); else glds_b7(g, off, l, l + 4096);
    }
    {
      unsigned ls[4][2];
#pragma unroll
      for (int j = 0; j < 4; ++j) {
        ls[j][0] = l0 + ((np + j) & (RING - 1)) * STG;
        ls[j][1] = ls[j][0] + 4096;
        asm volatile("" : "+s"(ls[j][0]), "+s"(ls[j][1]));
      }
      auto fast = [&](auto AC) {
        constexpr bool A = decltype(AC)::value;
        constexpr unsigned STEP = A ? 128 : STB;
        const char* g = base2 + (size_t)(np - ini) * STEP;
#pragma unroll 1
        for (int q = 0; n < nl; n += 4, ++q) {
          const char* gl = base2 + (size_t)(cls + 4 * ((((n + 3 - ini) >> 2) + ini) ^ hx)) * STEP;
          asm volatile("" : "+s"(gl));
#pragma unroll
          for (int j = 0; j < 4; ++j) {
            if (A) WAITV(16); else WAITV(14);
            BAR();
            if (A) glds_a8(j < 3 ? g : gl, off, ls[j][0], ls[j][1]); else glds_b7(j < 3 ? g : gl, off, ls[j][0], ls[j][1]);
            g += STEP;
            asm volatile("" : "+s"(g));
          }
          if (__builtin_expect(q == qflag, 0))
            asm volatile("s_mov_b32 m0, %2\n\ts_nop 0\n\tglobal_load_lds_dword %0, %1 sc1" :: "v"(f3o + (lane & 1) * 4), "s"(ws), "s"(ldsb + FLG) : "memory");
          if (__builtin_expect(q == qz, 0)) {
            const unsigned v = *(const LAS unsigned*)(lds + FLG + lane * 4);
            if (!__all(v == (unsigned)(tag >> ((lane & 1) * 32)))) wait_flags((GAS u64*)(ws + f3o), tag);
            const char* zb = ws + OFF_Z1 + (size_t)(2 * (4 * mi + lw) * 28 + ((7 * pj) >> 1)) * 1024;
            const int own = 2 * zg - ((7 * pj) >> 1);
#pragma unroll 1
            for (int i = 0; i < 4; ++i) {
              const int k = (own + 2 + (i & 1)) & 3;
              glds<true>(zb + ((i >> 1) * 28 + k) * 1024, lane * 16, ldsb + SPARE + lw * 8192 + ((i >> 1) * 4 + k) * 1024);
            }
          }
        }
#pragma unroll 1
        for (int k = 0; n < NT; ++n, ++k) {
          unsigned l = l0 + (n & (RING - 1)) * STG, l2 = l + 4096;
          asm volatile("" : "+s"(l), "+s"(l2));
          if (A) WAITV(16); else WAITV(14);
          BAR();
          if (A) glds_a8(g, off, l, l2); else glds_b7(g, off, l, l2);
          g += (k == 2 || k == 5) ? 2 * STEP : STEP;
          asm volatile("" : "+s"(g));
        }
      };
      if (isA) fast(std::true_type{}); else fast(std::false_type{});
    }
    const char* wo_base = (const char*)wout + (dg < 7 ? dg : 0) * 128 + (size_t)lw * 56 * 896;
#pragma unroll 1
    for (int c = 0; c < 4; ++c) {
      if (c < 3) WAITV(14); else WAITV(7);
      BAR();
      const char* g = wo_base + (size_t)(((c + rot3) & 3) * 224) * 896;
#pragma unroll
      for (int i = 0; i < 7; ++i) glds(g + i * (8 * 896), wo_off, ldsb + c * STG + lw * 7168 + i * 1024);
    }
    if (LPRIO) __builtin_amdgcn_s_setprio(0);
  } else {
  const int fi = lane & 15, fq = lane >> 4;
  const unsigned arow = 8 * (fi >> 2) + (fi & 3);
  const unsigned tq = fi >> 2, tp = lane & 3;
  const unsigned aoff0 = (32 * wid + arow) * 128 + ((fq ^ (fi >> 1)) << 4);
  const unsigned aoff1 = aoff0 ^ 64;
  const __amdgpu_buffer_rsrc_t rws = __builtin_amdgcn_make_buffer_rsrc(ws, 0, WS_USED, 0x00020000);
  asm volatile("" : "+s"(tag));
  tag += EPOCH_STEP;
  const int kb = 4 * mi + wid;
  unsigned braw[2] = {0, 0};
  u32x4 z1o[2][2];
  if (threadIdx.x < 3) ((LAS unsigned*)(lds + CNT))[threadIdx.x] = 0;
  if (spare) {
    const int lc = lane < 56 ? lane : 55;
    const int row0 = mi * 128 + (p >> 2) * 64 + wid * 16;
    const u32x4 gv = *(const u32x4*)(gw + lc * 8);
    u32x4 xv[16];
#pragma unroll
    for (int i = 0; i < 16; ++i) xv[i] = *(const u32x4*)(x + (size_t)(row0 + i) * D + lc * 8);
    SCHED();
    WAITL0();
    BAR();
    if (threadIdx.x == 0) __hip_atomic_store((GAS u64*)(ws + OFF_F1 + (zg * 16 + mi) * 128), tag, RLX_AGENT);
    float res = 0.f;
#pragma unroll
    for (int i = 0; i < 16; ++i) {
      float a = 0.f;
#pragma unroll
      for (int j = 0; j < 4; ++j) a += bflo(xv[i][j]) * bflo(gv[j]) + bfhi(xv[i][j]) * bfhi(gv[j]);
      a = lane < 56 ? a : 0.f;
      a = xsum(a);
      a += __builtin_bit_cast(float, __builtin_amdgcn_update_dpp(0, __builtin_bit_cast(int, a), 0x128, 0xf, 0xf, false));
      a += __builtin_bit_cast(float, __builtin_amdgcn_update_dpp(0, __builtin_bit_cast(int, a), 0x124, 0xf, 0xf, false));
      a += __builtin_bit_cast(float, __builtin_amdgcn_update_dpp(0, __builtin_bit_cast(int, a), 0x122, 0xf, 0xf, false));
      a += __builtin_bit_cast(float, __builtin_amdgcn_update_dpp(0, __builtin_bit_cast(int, a), 0x121, 0xf, 0xf, false));
      if (lane == i) res = a;
    }
    if (lane < 16) __hip_atomic_store((GAS float*)(ws + OFF_G) + row0 + lane, 1.0f / (1.0f + __expf(-res)), RLX_AGENT);
#pragma unroll
    for (int h = 0; h < 2; ++h) z1o[h][0] = z1o[h][1] = (u32x4){0, 0, 0, 0};
  } else {
    bf16x8 xf[14][2];
    f32x4 z2[2][4], ac[2][4];
    const unsigned fw = ((tq >> 1) & 1) | ((fq & 1) << 1);
    unsigned bw[4];
#pragma unroll
    for (int n = 0; n < 4; ++n) bw[n] = R1 + 16384 + (8 * fq + tq) * 128 + ((n ^ fw) << 5) + tp * 8;
    const unsigned ax0 = R1 + aoff0, ax1 = R1 + aoff1;
    {
      bf16x8 wf[2][2][4];
      auto rd = [&](int t) {
        const unsigned so = (t % 3) * R1S;
#pragma unroll
        for (int k = 0; k < 2; ++k) {
#pragma unroll
          for (int h = 0; h < 2; ++h) xf[2 * t + k][h] = *(const LAS bf16x8*)(lds + (k ? ax1 : ax0) + so + h * 512);
#pragma unroll
          for (int n = 0; n < 4; ++n) wf[t & 1][k][n] = CAT(TRD(lds + bw[n] + so + k * 4096), TRD(lds + bw[n] + so + k * 4096 + 512));
        }
      };
      auto mm = [&](int t) {
#pragma unroll
        for (int k = 0; k < 2; ++k)
#pragma unroll
          for (int n = 0; n < 4; ++n)
#pragma unroll
            for (int h = 0; h < 2; ++h) z2[h][n] = MFMA(wf[t & 1][k][n], xf[2 * t + k][h], z2[h][n]);
      };
#pragma unroll
      for (int h = 0; h < 2; ++h)
#pragma unroll
        for (int n = 0; n < 4; ++n) z2[h][n] = (f32x4){0, 0, 0, 0};
      WAITV(3);
      WAITL0();
      BAR();
      STAMP(1);
      rd(0);
      WAITV(0);
      braw[0] = bsp[mi * 128 + 32 * wid + arow];
      braw[1] = bsp[mi * 128 + 32 * wid + arow + 4];
#pragma unroll
      for (int t = 0; t < 6; ++t) {
        SCHED();
        WAITL0(); BAR();
        rd(t + 1); mm(t);
#pragma unroll
        for (int i = 0; i < 10; ++i) { SGB(0x8, 1); SGB(0x100, 2); }
        SGB(0x8, 6);
      }
      SCHED();
      mm(6);
    }
    STAMP(2);
    {
      float mean_p[2], m2_p[2];
#pragma unroll
      for (int h = 0; h < 2; ++h) {
        f32x4 s4 = {0, 0, 0, 0};
#pragma unroll
        for (int n = 0; n < 4; ++n) { z2[h][n] = gelu4(z2[h][n]); s4 += z2[h][n]; }
        mean_p[h] = xsum((s4[0] + s4[1]) + (s4[2] + s4[3])) * (1.0f / 64.0f);
      }
#pragma unroll
      for (int h = 0; h < 2; ++h) {
        f32x4 q4 = {0, 0, 0, 0};
#pragma unroll
        for (int n = 0; n < 4; ++n) { const f32x4 d = z2[h][n] - mean_p[h]; q4 += d * d; }
        m2_p[h] = xsum((q4[0] + q4[1]) + (q4[2] + q4[3]));
      }
      const float vm = (fq & 1) ? mean_p[1] : mean_p[0], vq = (fq & 1) ? m2_p[1] : m2_p[0];
      const unsigned tl = (unsigned)(tag << 24), th = (unsigned)(tag >> 8);
      asm volatile("" : "+v"(braw[0]), "+v"(braw[1]));
      if (lane < 32)
        st128(rws, (u32x4){tl | ((__float_as_uint(vm) + 0x80u) >> 8), th, tl | ((__float_as_uint(vq) + 0x80u) >> 8), th},
              lane * 16 + OFF_X + (kb * 14 + zg) * 512);
    }
    SCHED();
    STAMP(3);
    BAR();
    STAMP(4);
    {
      bf16x8 df[14][4];
      const unsigned d0 = fi * 128 + ((fq ^ (fi >> 1)) << 4), d1 = d0 ^ 64;
      auto rdd = [&](int ks) {
#pragma unroll
        for (int n = 0; n < 4; ++n) df[ks][n] = *(const LAS bf16x8*)(lds + ((ks & 1) ? d1 : d0) + (ks >> 1) * 8192 + n * 2048);
      };
      rdd(0); rdd(1);
#pragma unroll
      for (int h = 0; h < 2; ++h)
#pragma unroll
        for (int n = 0; n < 4; ++n) ac[h][n] = (f32x4){0, 0, 0, 0};
      SCHED();
#pragma unroll
      for (int ks = 0; ks < 14; ++ks) {
        if (ks + 2 < 14) rdd(ks + 2);
#pragma unroll
        for (int n = 0; n < 4; ++n)
#pragma unroll
          for (int h = 0; h < 2; ++h) ac[h][n] = MFMA(xf[ks][h], df[ks][n], ac[h][n]);
        if (ks + 2 < 14) {
#pragma unroll
          for (int i = 0; i < 4; ++i) { SGB(0x8, 2); SGB(0x100, 1); }
        }
        SCHED();
      }
      {
#pragma unroll
        for (int n = 0; n < 4; ++n) {
          const u32x4 o = {pk_bf16(ac[0][n][0], ac[0][n][1]), pk_bf16(ac[0][n][2], ac[0][n][3]),
                           pk_bf16(ac[1][n][0], ac[1][n][1]), pk_bf16(ac[1][n][2], ac[1][n][3])};
          st128(rws, o, lane * 16 + bt_pos(4 * zg + n, kb));
        }
      }
    }
    SCHED();
    f32x4 zt[4][2];
    unsigned zo[4];
    {
      LAS char* sc = lds + SCR + wid * 4096;
#pragma unroll
      for (int h = 0; h < 2; ++h)
#pragma unroll
        for (int n = 0; n < 4; ++n)
          *(LAS u32x2*)(sc + (arow + 4 * h) * 128 + (((4 * n + fq) ^ fi) << 3)) =
              (u32x2){pk_bf16(z2[h][n][0], z2[h][n][1]), pk_bf16(z2[h][n][2], z2[h][n][3])};
      WAITL0();
#pragma unroll
      for (int n = 0; n < 4; ++n) {
        const unsigned a = (8 * fq + tq) * 128 + (((4 * n + tp) ^ ((fq << 2) | tq)) << 3);
#pragma unroll
        for (int e = 0; e < 2; ++e) {
          const bf16x4 v4 = TRD(sc + a + e * 512);
          const u32x2 v = *(const u32x2*)&v4;
          zt[n][e] = (f32x4){bflo(v[0]), bfhi(v[0]), bflo(v[1]), bfhi(v[1])};
          asm volatile("" : "+v"(zt[n][e]));
        }
        zo[n] = lane * 16 + bt_pos(56 + 4 * zg + n, kb);
        asm volatile("" : "+v"(zo[n]));
      }
    }
    SCHED();
    STAMP(5);
    BAR();
    STAMP(6);
    bf16x8 wf[14][4];
    auto rdw = [&](int ks) {
#pragma unroll
      for (int n = 0; n < 4; ++n) {
        LAS char* a = lds + (bw[n] - (R1 + 16384 - W1C0)) + (ks < 2 ? 0 : W1 - W1C0) + ks * 4096;
        wf[ks][n] = CAT(TRD(a), TRD(a + 512));
      }
    };
    {
      f32x4 rs[2], mu[2];
#pragma unroll
      for (int e = 0; e < 2; ++e) {
        rs[e] = *(const LAS f32x4*)(lds + STAT + wid * 256 + fq * 32 + e * 16);
        mu[e] = *(const LAS f32x4*)(lds + STAT + wid * 256 + 128 + fq * 32 + e * 16);
      }
      SCHED();
      rdw(0); rdw(1);
      SCHED();
#pragma unroll
      for (int n = 0; n < 4; ++n) {
        const f32x4 v = zt[n][0] * rs[0] + mu[0], w = zt[n][1] * rs[1] + mu[1];
        st128(rws, (u32x4){pk_bf16(v[0], v[1]), pk_bf16(v[2], v[3]), pk_bf16(w[0], w[1]), pk_bf16(w[2], w[3])}, zo[n]);
      }
    }
#pragma unroll
    for (int h = 0; h < 2; ++h)
#pragma unroll
      for (int n = 0; n < 4; ++n) ac[h][n] = (f32x4){0, 0, 0, 0};
    SCHED();
    STAMP(7);
#pragma unroll
    for (int ks = 0; ks < 14; ++ks) {
      if (ks + 2 < 14) rdw(ks + 2);
#pragma unroll
      for (int n = 0; n < 4; ++n)
#pragma unroll
        for (int h = 0; h < 2; ++h) ac[h][n] = MFMA(wf[ks][n], xf[ks][h], ac[h][n]);
      if (ks + 2 < 14) {
#pragma unroll
        for (int i = 0; i < 8; ++i) { SGB(0x8, 1); SGB(0x100, 1); }
      }
      SCHED();
      if (ks == FLAGKS) { arrive_and_flag(lds, 0, ws, OFF_F1 + (zg * 16 + mi) * 128, tag, lane); SCHED(); STAMP(8); }
    }
#pragma unroll
    for (int h = 0; h < 2; ++h)
#pragma unroll
      for (int k = 0; k < 2; ++k) {
        const f32x4 v = gelu4(ac[h][2 * k]), w = gelu4(ac[h][2 * k + 1]);
        z1o[h][k] = (u32x4){pk_bf16(v[0], v[1]), pk_bf16(v[2], v[3]), pk_bf16(w[0], w[1]), pk_bf16(w[2], w[3])};
      }
  }
  f32x4 acc[2][7];
#pragma unroll
  for (int m = 0; m < 2; ++m) {
    const float bv = G ? bflo(braw[m]) : 0.f;
#pragma unroll
    for (int n = 0; n < 7; ++n) { acc[m][n] = (f32x4){bv, bv, bv, bv}; asm volatile("" : "+v"(acc[m][n])); }
  }
  u32x4 zq[2][3];
  u32x2 zs[2];
  {
    const unsigned boff = STA + lane * 16;
    bf16x8 fa[2][2][2], fb[2][2][7];
    unsigned ba[2][2] = {{aoff0, aoff1}, {aoff0 + 2 * STG, aoff1 + 2 * STG}}, bb[2] = {boff, boff + 2 * STG};
    asm volatile("" : "+v"(ba[1][0]), "+v"(ba[1][1]), "+v"(bb[1]));
    auto rd = [&](int set, int slot) {
      const int so = (slot & 1) * STG;
#pragma unroll
      for (int k = 0; k < 2; ++k) {
#pragma unroll
        for (int m = 0; m < 2; ++m) fa[set][k][m] = *(const LAS bf16x8*)(lds + ba[slot >> 1][k] + (so + m * 512));
#pragma unroll
        for (int n = 0; n < 7; ++n) fb[set][k][n] = *(const LAS bf16x8*)(lds + bb[slot >> 1] + (so + k * 7168 + n * 1024));
      }
    };
    auto mm = [&](int set) {
#pragma unroll
      for (int k = 0; k < 2; ++k)
#pragma unroll
        for (int n = 0; n < 7; ++n)
#pragma unroll
          for (int m = 0; m < 2; ++m) acc[m][n] = MFMA(fb[set][k][n], fa[set][k][m], acc[m][n]);
    };
    auto sched = [&]() {
#pragma unroll
      for (int i = 0; i < 18; ++i) { SGB(0x8, 1); SGB(0x100, 1); }
      SGB(0x8, 10);
      SCHED();
    };
    STAMP(9);
    WAITL0();
    BAR();
    STAMP(10);
    CSTAMP(17);
    rd(0, 0);
    if (G) {
      LAS char* zl = lds + SPARE + wid * 8192 + (2 * zg - ((7 * (p - 8)) >> 1)) * 1024 + lane * 16;
#pragma unroll
      for (int j = 0; j < 4; ++j) *(LAS u32x4*)(zl + ((j >> 1) * 4 + (j & 1)) * 1024) = z1o[j >> 1][j & 1];
    } else {
      if (!spare) {
#pragma unroll
        for (int j = 0; j < 4; ++j) st128(rws, z1o[j >> 1][j & 1], lane * 16 + OFF_Z1 + ((2 * kb + (j >> 1)) * 28 + 2 * zg + (j & 1)) * 1024);
      }
#pragma unroll
      for (int j = 0; j < 8; ++j)
        *(LAS u32x4*)(lds + SPARE + wid * 8192 + j * 1024 + lane * 16) = (u32x4){0x3f803f80u, 0x3f803f80u, 0x3f803f80u, 0x3f803f80u};
    }
    SCHED();
#pragma unroll 1
    for (int t = 0; t < 7; ++t) {
#define QS(i)
      WAITL0(); QS(1); BAR(); QS(2); rd(1, 1); mm(0); sched();
      WAITL0(); QS(3); BAR(); QS(4); rd(0, 2); mm(1); sched();
      WAITL0(); QS(5); BAR(); QS(6); rd(1, 3); mm(0); sched();
      WAITL0(); QS(7); BAR(); QS(8); rd(0, 0); mm(1); sched();
      QS(9);
      if (__builtin_expect(t == 0 && !G, 0)) arrive_and_flag(lds, 1, ws, OFF_F3 + (mi * 16 + zg) * 128, tag, lane);
    }
    WAITL0(); BAR(); rd(1, 1); mm(0); sched();
    WAITL0(); BAR(); rd(0, 2); mm(1); sched();
    WAITL0(); BAR(); rd(1, 3); mm(0); sched();
    WAITL0(); BAR();
    {
      const int odd = p & 1;
      LAS char* zl = lds + SPARE + wid * 8192 + lane * 16;
#pragma unroll
      for (int m = 0; m < 2; ++m) {
#pragma unroll
        for (int k = 0; k < 3; ++k) zq[m][k] = *(const LAS u32x4*)(zl + odd * 1024 + (m * 4 + k) * 1024);
        zs[m] = *(const LAS u32x2*)(zl + (odd ? 8 : 3072) + m * 4096);
      }
    }
    auto fin = [&](f32x4 v, unsigned z0, unsigned z1) {
      return (u32x2){pk_bf16(v[0] * bflo(z0), v[1] * bfhi(z0)), pk_bf16(v[2] * bflo(z1), v[3] * bfhi(z1))};
    };
    const unsigned o2 = lane * 16 + OFF_O2 + (2 * kb * 48 + p * 3) * 1024;
#pragma unroll
    for (int g = 0; g < 5; ++g) {
      if (g < 4) {
#pragma unroll
        for (int k = 0; k < 2; ++k)
#pragma unroll
          for (int n = 2 * g; n < 2 * g + 2 && n < 7; ++n)
#pragma unroll
            for (int m = 0; m < 2; ++m) acc[m][n] = MFMA(fb[1][k][n], fa[1][k][m], acc[m][n]);
      }
      if (g == 4) {
        const u32x2 lo = fin(acc[0][6], zs[0][0], zs[0][1]), hi = fin(acc[1][6], zs[1][0], zs[1][1]);
        st128(rws, (u32x4){lo[0], lo[1], hi[0], hi[1]}, lane * 16 + OFF_O1 + (kb * 16 + p) * 1024);
      } else if (g > 0) {
        const int k = g - 1;
#pragma unroll
        for (int m = 0; m < 2; ++m) {
          const u32x2 lo = fin(acc[m][2 * k], zq[m][k][0], zq[m][k][1]), hi = fin(acc[m][2 * k + 1], zq[m][k][2], zq[m][k][3]);
          st128(rws, (u32x4){lo[0], lo[1], hi[0], hi[1]}, o2 + (m * 48 + k) * 1024);
        }
#pragma unroll
        for (int i = 0; i < (g < 3 ? 8 : 4); ++i) { SGB(0x8, 1); SGB(0x2, 4); }
      }
      SCHED();
      if (g == 3) { STAMP(11); CSTAMP(18); }
    }
  }
  STAMP(12);
  }
  {
    int ln = threadIdx.x;
    asm volatile("" : "+v"(ln));
    ln &= 63;
    const int fi = ln & 15, fq = ln >> 4, kh = 1 - (wid >> 2);
    const unsigned tq = fi >> 2, tp = ln & 3;
    const int mt = 4 * rg + (wid & 3);
    const __amdgpu_buffer_rsrc_t rws = __builtin_amdgcn_make_buffer_rsrc(ws, 0, WS_USED, 0x00020000);
    unsigned wb[4];
#pragma unroll
    for (int n = 0; n < 4; ++n) wb[n] = kh * 2 * STG + (4 * fq + tq) * 128 + ((n ^ (((4 * fq + tq) >> 1) & 3)) << 5) + tp * 8;
    bf16x8 wf[14][4];
    auto rdw = [&](int i) {
#pragma unroll
      for (int n = 0; n < 4; ++n) {
        LAS char* a = lds + wb[n] + ((i / 7) * STG + (i % 7) * 4096);
        wf[i][n] = CAT(TRD(a), TRD(a + 2048));
      }
    };
    constexpr int PRE = 11;
    unsigned h2[2], h1[2];
#pragma unroll
    for (int c = 0; c < 2; ++c) {
      const int pp = 8 + 2 * ((2 * kh + c + rot3) & 3);
      h2[c] = OFF_O2 + (mt * 48 + pp * 3) * 1024;
      h1[c] = OFF_O1 + ((mt >> 1) * 16 + pp) * 1024 + (mt & 1) * 8;
      asm volatile("" : "+s"(h2[c]), "+s"(h1[c]));
    }
    const int t = 32 * (mt >> 1) + 8 * tq + (fi & 3) + 4 * (mt & 1);
    const int P = 2 * dg + kh, q = P / 7, k7 = P % 7;
    const bool split = k7 == 3;
    unsigned yo = split ? OFF_O1 + ((mt >> 1) * 16 + 2 * q) * 1024 : OFF_O2 + (mt * 48 + q * 6 + (k7 < 3 ? k7 : k7 - 1)) * 1024;
    unsigned yo2 = yo + (split ? 4096 : 12288), go = OFF_G + t * 4, l16 = ln * 16;
    unsigned oo = (t * D + 64 * dg + 32 * kh + 4 * fq) * 2;
    asm volatile("" : "+s"(yo), "+s"(yo2));
    asm volatile("" : "+v"(go), "+v"(l16), "+v"(oo));
    const unsigned blk = (dg < 7 ? dg : 0) * 16 + wid * 2 + (fi >> 3);
    unsigned to = (blk < 96 ? OFF_O2 + ((4 * rg + blk / 24) * 48 + 24 + blk % 24) * 1024
                            : OFF_O1 + ((2 * rg + ((blk - 96) >> 3)) * 16 + 8 + (blk & 7)) * 1024) + (fi & 7) * 128;
    if (HTOUCH) asm volatile("" : "+v"(to));
    f32x4 oc[4];
#pragma unroll
    for (int n = 0; n < 4; ++n) { oc[n] = (f32x4){0, 0, 0, 0}; asm volatile("" : "+v"(oc[n])); }
#pragma unroll
    for (int n = 0; n < 4; ++n) asm volatile("" : "+v"(wb[n]));
    SCHED();
    if (wid < 4) BAR();
#pragma unroll
    for (int i = 0; i < PRE1; ++i) rdw(i);
    SCHED();
    if (wid >= 4) {
      WAITV(0);
      STAMP(5);
      BAR();
    } else {
      arrive_and_flag(lds, 2, ws, OFF_F2 + (mi * 16 + p) * 128, tag, ln);
      STAMP(13);
    }
    if (r >= 28) { STAMP_FLUSH(); return; }
#pragma unroll
    for (int i = PRE1; i < PRE; ++i) rdw(i);
    SCHED();
    GAS u64* f2 = (GAS u64*)(ws + OFF_F2 + ((rg >> 1) * 16 + fi) * 128);
    if (wid == 4) {
#pragma unroll 1
      for (unsigned spins = 0; spins < SPIN_CAP && PHOLD; ++spins) {
        if (__hip_atomic_load((LAS unsigned*)(lds + CNT) + 2, __ATOMIC_RELAXED, __HIP_MEMORY_SCOPE_WORKGROUP) == 4) break;
        __builtin_amdgcn_s_sleep(1);
      }
      __builtin_amdgcn_s_sleep(PSLEEP);
      wait_flags(f2, tag);
      if (b == 0 && ln == 0) __hip_atomic_store((GAS u64*)(ws + OFF_E), tag, RLX_AGENT);
    }
    BAR();
    STAMP(14);
    u32x4 hf[14];
    auto ldh = [&](int i) {
      const int c = i / 7, k = i % 7;
      if (k == 3) {
        const u32x2 lo = __builtin_amdgcn_raw_buffer_load_b64(rws, l16, h1[c], 16);
        const u32x2 hi = __builtin_amdgcn_raw_buffer_load_b64(rws, l16 + 1024, h1[c], 16);
        hf[i] = (u32x4){lo[0], lo[1], hi[0], hi[1]};
      } else {
        hf[i] = __builtin_amdgcn_raw_buffer_load_b128(rws, l16 + (k < 3 ? k : k - 4) * 1024, h2[c] + (k < 3 ? 0 : 3072), 16);
      }
    };
#pragma unroll
    for (int i = 0; i < HAH; ++i) {
      if (HTOUCH && i == HTOUCH - 1)
        asm volatile("s_mov_b32 m0, %2\n\ts_nop 0\n\tglobal_load_lds_dword %0, %1 sc1" :: "v"(to), "s"(ws), "s"(ldsb + JUNK) : "memory");
      ldh(i);
    }
    u32x4 ya, yb, yc, yd2;
    float g;
    SCHED();
#pragma unroll
    for (int i = 0; i < 14; ++i) {
      if (i + HAH < 14) ldh(i + HAH);
      if (i + HAH == 14) {
        ya = __builtin_amdgcn_raw_buffer_load_b128(rws, l16, yo, 16);
        yb = __builtin_amdgcn_raw_buffer_load_b128(rws, l16, yo2, 16);
        g = __uint_as_float(__builtin_amdgcn_raw_buffer_load_b32(rws, go, 0, 16));
        yc = yd2 = (u32x4){0, 0, 0, 0};
        if (split) {
          yc = __builtin_amdgcn_raw_buffer_load_b128(rws, l16 + 1024, yo, 16);
          yd2 = __builtin_amdgcn_raw_buffer_load_b128(rws, l16 + 1024, yo2, 16);
        }
      }
      SCHED();
#pragma unroll
      for (int n = 0; n < 4; ++n) oc[n] = MFMA(wf[i][n], *(const bf16x8*)&hf[i], oc[n]);
      SCHED();
      if (i + PRE < 14) rdw(i + PRE);
      SCHED();
    }
    STAMP(15);
    LAS char* ex = lds + SPARE + ln * 16;
    *(LAS f32x4*)(ex + wid * 2048) = kh ? oc[0] : oc[2];
    *(LAS f32x4*)(ex + wid * 2048 + 1024) = kh ? oc[1] : oc[3];
    WAITL0();
    BAR();
    const float g1 = 1.0f - g;
    u32x4 yr = ya, yi = yb;
    if (split) {
      const bool up = mt & 1;
      yr = up ? (u32x4){ya[2], ya[3], yc[2], yc[3]} : (u32x4){ya[0], ya[1], yc[0], yc[1]};
      yi = up ? (u32x4){yb[2], yb[3], yd2[2], yd2[3]} : (u32x4){yb[0], yb[1], yd2[0], yd2[1]};
    }
#pragma unroll
    for (int n = 0; n < 2; ++n) {
      const f32x4 o = *(const LAS f32x4*)(ex + (wid ^ 4) * 2048 + n * 1024) + (kh ? oc[2 + n] : oc[n]);
      const f32x4 y = {bflo(yr[2 * n]) - bflo(yi[2 * n]), bfhi(yr[2 * n]) - bfhi(yi[2 * n]),
                       bflo(yr[2 * n + 1]) - bflo(yi[2 * n + 1]), bfhi(yr[2 * n + 1]) - bfhi(yi[2 * n + 1])};
      const f32x4 v = y * g + o * g1;
      *(u32x2*)((char*)out + oo + 32 * n) = (u32x2){pk_bf16(v[0], v[1]), pk_bf16(v[2], v[3])};
    }
  }
  STAMP(16);
  STAMP_FLUSH();
}

extern "C" void kernel_launch(void* const* d_in, const int* in_sizes, int n_in, void* d_out, int out_size, void* d_ws,
                              size_t ws_size, hipStream_t stream) {
  fused<<<256, 512, 0, stream>>>((const u16*)d_in[0], (const u16*)d_in[1], (const u16*)d_in[2], (const u16*)d_in[3],
                                 (const u16*)d_in[4], (const u16*)d_in[6], (const u16*)d_in[7], (const u16*)d_in[8],
                                 (const u16*)d_in[9], (const u16*)d_in[10], (char*)d_ws, (u16*)d_out);
}
```

```cpp
#include <hip/hip_runtime.h>
#include <hip/hip_bf16.h>
#include <type_traits>

typedef unsigned short u16;
typedef unsigned long long u64;
typedef short bf16x8 __attribute__((ext_vector_type(8)));
typedef short bf16x4 __attribute__((ext_vector_type(4)));
typedef float f32x2 __attribute__((ext_vector_type(2)));
typedef float f32x4 __attribute__((ext_vector_type(4)));
typedef unsigned u32x2 __attribute__((ext_vector_type(2)));
typedef unsigned u32x4 __attribute__((ext_vector_type(4)));
#define LAS __attribute__((address_space(3)))
#define GAS __attribute__((address_space(1)))
#define RLX_AGENT __ATOMIC_RELAXED, __HIP_MEMORY_SCOPE_AGENT

#define WAITV(n) asm volatile("s_waitcnt vmcnt(%0)" ::"n"(n) : "memory")
#define WAITL0() do { asm volatile("" ::: "memory"); __builtin_amdgcn_s_waitcnt(0xC07F); asm volatile("" ::: "memory"); } while (0)
#define BAR() do { asm volatile("" ::: "memory"); __builtin_amdgcn_s_barrier(); asm volatile("" ::: "memory"); } while (0)
#define SCHED() __builtin_amdgcn_sched_barrier(0)
#define SGB(mask, n) __builtin_amdgcn_sched_group_barrier(mask, n, 0)
#define MFMA(a, b, c) __builtin_amdgcn_mfma_f32_16x16x32_bf16(a, b, c, 0, 0, 0)
#define TRD(p) __builtin_amdgcn_ds_read_tr16_b64_v4i16((LAS bf16x4*)(p))
#define CAT(lo, hi) __builtin_shufflevector(lo, hi, 0, 1, 2, 3, 4, 5, 6, 7)

template <bool SC1 = false>
__device__ __forceinline__ void glds(const void* sbase, unsigned voff, unsigned l) {
  l = __builtin_amdgcn_readfirstlane(l);
  if (SC1) asm volatile("s_mov_b32 m0, %2\n\ts_nop 0\n\tglobal_load_lds_dwordx4 %0, %1 sc1" :: "v"(voff), "s"(sbase), "s"(l) : "memory");
  else asm volatile("s_mov_b32 m0, %2\n\ts_nop 0\n\tglobal_load_lds_dwordx4 %0, %1" :: "v"(voff), "s"(sbase), "s"(l) : "memory");
}
__device__ __forceinline__ void glds_a8(const void* g, const unsigned (&o)[8], unsigned l0, unsigned l1) {
  asm volatile("s_mov_b32 m0, %9\n\ts_nop 0\n\t"
               "global_load_lds_dwordx4 %0, %8\n\tglobal_load_lds_dwordx4 %1, %8 offset:1024\n\t"
               "global_load_lds_dwordx4 %2, %8 offset:2048\n\tglobal_load_lds_dwordx4 %3, %8 offset:3072\n\t"
               "s_mov_b32 m0, %10\n\ts_nop 0\n\t"
               "global_load_lds_dwordx4 %4, %8\n\tglobal_load_lds_dwordx4 %5, %8 offset:1024\n\t"
               "global_load_lds_dwordx4 %6, %8 offset:2048\n\tglobal_load_lds_dwordx4 %7, %8 offset:3072"
               :: "v"(o[0]), "v"(o[1]), "v"(o[2]), "v"(o[3]), "v"(o[4]), "v"(o[5]), "v"(o[6]), "v"(o[7]), "s"(g), "s"(l0), "s"(l1) : "memory");
}
__device__ __forceinline__ void glds_b7(const void* g, const unsigned (&o)[8], unsigned l0, unsigned l1) {
  asm volatile("s_mov_b32 m0, %3\n\ts_nop 0\n\t"
               "global_load_lds_dwordx4 %0, %2 sc1\n\tglobal_load_lds_dwordx4 %0, %2 offset:1024 sc1\n\t"
               "global_load_lds_dwordx4 %0, %2 offset:2048 sc1\n\tglobal_load_lds_dwordx4 %0, %2 offset:3072 sc1\n\t"
               "s_mov_b32 m0, %4\n\ts_nop 0\n\t"
               "global_load_lds_dwordx4 %1, %2 sc1\n\tglobal_load_lds_dwordx4 %1, %2 offset:1024 sc1\n\t"
               "global_load_lds_dwordx4 %1, %2 offset:2048 sc1"
               :: "v"(o[0]), "v"(o[4]), "s"(g), "s"(l0), "s"(l1) : "memory");
}
__device__ __forceinline__ void touch(const void* sbase, unsigned voff, unsigned l) {
  l = __builtin_amdgcn_readfirstlane(l);
  asm volatile("s_mov_b32 m0, %2\n\ts_nop 0\n\tglobal_load_lds_dword %0, %1" :: "v"(voff), "s"(sbase), "s"(l) : "memory");
}

constexpr int S = 2048, D = 448;

__device__ __forceinline__ float bflo(unsigned v) { return __uint_as_float(v << 16); }
__device__ __forceinline__ float bfhi(unsigned v) { return __uint_as_float(v & 0xffff0000u); }
__device__ __forceinline__ unsigned pk_bf16(float lo, float hi) {
  typedef __bf16 bf2 __attribute__((ext_vector_type(2)));
  f32x2 v = {lo, hi};
  bf2 b = __builtin_convertvector(v, bf2);
  return *(unsigned*)&b;
}
__device__ __forceinline__ f32x2 gelu2(f32x2 v) {
  const float c0 = -2.0f * 0.7978845608028654f * 1.4426950408889634f;
  const float c1 = c0 * 0.044715f;
  const f32x2 u = v * ((v * v) * c1 + c0);
  f32x2 e = {__builtin_amdgcn_exp2f(u.x), __builtin_amdgcn_exp2f(u.y)};
  e = e + 1.0f;
  const f32x2 rc = {__builtin_amdgcn_rcpf(e.x), __builtin_amdgcn_rcpf(e.y)};
  return v * rc;
}
__device__ __forceinline__ f32x4 gelu4(f32x4 v) {
  const f32x2 lo = gelu2((f32x2){v[0], v[1]}), hi = gelu2((f32x2){v[2], v[3]});
  return (f32x4){lo.x, lo.y, hi.x, hi.y};
}
__device__ __forceinline__ float xsum(float s) {
  auto a = __builtin_amdgcn_permlane16_swap(__float_as_uint(s), __float_as_uint(s), false, false);
  s = __uint_as_float(a[0]) + __uint_as_float(a[1]);
  auto b = __builtin_amdgcn_permlane32_swap(__float_as_uint(s), __float_as_uint(s), false, false);
  return __uint_as_float(b[0]) + __uint_as_float(b[1]);
}

constexpr unsigned OFF_BT = 0;
constexpr unsigned OFF_O2 = OFF_BT + 1792 * S * 2;
constexpr unsigned OFF_O1 = OFF_O2 + 128 * 48 * 1024;
constexpr unsigned OFF_Z1 = OFF_O2 + 1792 * S * 2;
constexpr unsigned OFF_G = OFF_Z1 + 896 * S * 2;
constexpr unsigned OFF_X = OFF_G + 8192;
constexpr unsigned OFF_F1 = OFF_X + 64 * 14 * 512;
constexpr unsigned OFF_F2 = OFF_F1 + 256 * 128;
constexpr unsigned OFF_F3 = OFF_F2 + 256 * 128;
constexpr unsigned OFF_E = OFF_F3 + 256 * 128;
constexpr unsigned WS_USED = OFF_E + 128;
constexpr u64 EPOCH_STEP = 0x9E3779B97F4A7C15ull;
constexpr unsigned CODE_BYTES = 28 * 1024;
constexpr unsigned SPIN_CAP = 1u << 21;

constexpr unsigned R1 = 89600, R1S = 24576, SCR = 89600, W1 = 105984;
constexpr int STA = 16384, STB = 14336, STG = STA + STB, RING = 4, NT = 32;
constexpr unsigned SPARE = RING * STG;
constexpr unsigned W1C0 = 77824;
constexpr unsigned STAT = 86016, FLG = 155648, CNT = 163328, JUNK = 163584;
__device__ __forceinline__ constexpr unsigned slot1(int t) { return t == 3 ? 57344u : R1 + ((t + 1 - (t > 3)) % 3) * R1S; }
__device__ __forceinline__ unsigned bt_pos(int T, int kb) {
  const int pp = T / 7, n7 = T % 7, j = (pp & 1) ? (n7 + 6) % 7 : n7;
  return OFF_BT + ((pp * 64 + kb) * 7 + j) * 1024;
}

__device__ __forceinline__ void st128(__amdgpu_buffer_rsrc_t rs, u32x4 v, unsigned off) {
  __builtin_amdgcn_raw_buffer_store_b128(v, rs, off, 0, 16);
}
__device__ __forceinline__ void wait_flags(GAS u64* p, u64 tag) {
#pragma unroll 1
  for (unsigned spins = 0; spins < SPIN_CAP; ++spins) {
    const u64 v = __hip_atomic_load(p, RLX_AGENT);
    if (__all(v == tag)) break;
    __builtin_amdgcn_s_sleep(2);
  }
  __builtin_amdgcn_fence(__ATOMIC_ACQUIRE, "wavefront");
}

__device__ __forceinline__ void arrive_and_flag(LAS char* lds, int which, char* ws, unsigned flag_off, u64 tag, int lane) {
  WAITV(0);
  if (lane == 0) {
    const unsigned old = __hip_atomic_fetch_add((LAS unsigned*)(lds + CNT) + which, 1u, __ATOMIC_RELAXED, __HIP_MEMORY_SCOPE_WORKGROUP);
    if (old == 3) __hip_atomic_store((GAS u64*)(ws + flag_off), tag, RLX_AGENT);
  }
}

__global__ __launch_bounds__(512, 2) void fused(const u16* __restrict__ x, const u16* __restrict__ wsr, const u16* __restrict__ wsi,
                                                const u16* __restrict__ wdr, const u16* __restrict__ wdi, const u16* __restrict__ win,
                                                const u16* __restrict__ wsp, const u16* __restrict__ bsp, const u16* __restrict__ wout,
                                                const u16* __restrict__ gw, char* __restrict__ ws, u16* __restrict__ out) {
  __shared__ __attribute__((aligned(1024))) char smem[163840];
  LAS char* lds = (LAS char*)smem;
  const int wid = __builtin_amdgcn_readfirstlane(threadIdx.x >> 6), lane = threadIdx.x & 63;
  const int b = blockIdx.x, xcd = b & 7, r = b >> 3;
  const bool G = xcd < 4;
  const int ib = G ? (r & 3) : (r & 7), ia = G ? (r >> 2) : (r >> 3);
  const int mi = (G ? xcd * 4 : (xcd & 1) * 8) + ib;
  const int p = (G ? 8 : (xcd & 2) * 2) + ia;
  const int cls = (ia + ib) & 3;
  const bool spare = (p & 11) == 3;
  const int zg = (int)((0xdb976420fca8e531ull >> (4 * p)) & 15);
  const unsigned o7 = (cls & 2) ? ((cls & 1) ? 0x6423501u : 0x6542031u) : ((cls & 1) ? 0x6532410u : 0x5463120u);
  const unsigned ldsb = __builtin_amdgcn_readfirstlane((unsigned)(size_t)lds);
  if (!spare) {
    const unsigned l3 = lane >> 3, l7 = lane & 7, l4 = (lane >> 4) & 1;
    const char* xg = (const char*)x + (size_t)(mi * 128 + wid * 16) * 896;
    const unsigned xo0 = l3 * 896 + ((l7 ^ (l4 | (((2 * wid) & 3) << 1))) << 4);
    const unsigned xo1 = (xo0 ^ 32) + 8 * 896;
    const char* wg = (const char*)win + (size_t)(wid * 8) * 3584 + 1792 + zg * 128;
    const unsigned wo = l3 * 3584 + (((((l7 >> 1) ^ (l4 | ((wid & 1) << 1))) << 1) | (l7 & 1)) << 4);
#pragma unroll
    for (int s = 0; s < 2; ++s) {
      const unsigned st = (o7 >> (4 * s)) & 7;
      glds(xg + st * 128, xo0, ldsb + slot1(s) + wid * 2048);
      glds(xg + st * 128, xo1, ldsb + slot1(s) + wid * 2048 + 1024);
      glds(wg + st * (64 * 3584), wo, ldsb + slot1(s) + 16384 + wid * 1024);
      if (s == 0) __builtin_amdgcn_s_sleep(3);
    }
  }
  if (wid == 7) {
    u64 pc;
    asm volatile("s_getpc_b64 %0" : "=s"(pc));
    const unsigned o = (r * 8 + (lane & 7)) * 128;
    touch((const char*)(pc & ~127ull), o < CODE_BYTES ? o : 0, ldsb + JUNK);
  }
  u64 tag = *(const __attribute__((address_space(4))) u64*)(ws + OFF_E);
  const int rg = 4 * xcd + (r & 3), dg = r >> 2, rot3 = (dg + r) & 3;

  if (wid >= 4) {
    const int lw = wid - 4;
    int lane = threadIdx.x;
    asm volatile("" : "+v"(lane));
    lane &= 63;
    const unsigned l3 = lane >> 3, l7 = lane & 7, l4 = (lane >> 4) & 1;
    const u16* am = p < 4 ? wsr : wsi;
    am = G ? wsp : am;
    asm volatile("" : "+s"(tag));
    tag += EPOCH_STEP;
    const unsigned pwo = (lane >> 2) * 4096 + (lane & 3) * 128;
    const char* xg = (const char*)x + (size_t)(mi * 128 + lw * 32) * 896;
    const unsigned xo = l3 * 896 + ((l7 ^ l4) << 4);
    const char* wg = (const char*)win + (size_t)(lw * 16) * 3584 + (zg < 14 ? zg : 0) * 128;
    const unsigned wo = l3 * 3584 + (((((l7 >> 1) ^ l4) << 1) | (l7 & 1)) << 4);
    auto stage1 = [&](int s) {
      const unsigned st = (o7 >> (4 * s)) & 7, l = ldsb + slot1(s);
#pragma unroll
      for (int i = 0; i < 4; ++i) glds(xg + st * 128 + i * (8 * 896), xo ^ (i << 5), l + lw * 4096 + i * 1024);
#pragma unroll
      for (int i = 0; i < 2; ++i) glds(wg + st * (64 * 3584) + i * (8 * 3584) + 1792, wo ^ (i << 6), l + 16384 + lw * 2048 + i * 1024);
    };
    if (!spare) stage1(2);
    WAITV(9);
    BAR();
    if (!spare) stage1(3);
    SCHED();
    if (spare) {
      const unsigned u = (mi * 2 + (p >> 2)) * 4 + lw, ul = u * 49 + (lane < 49 ? lane : 48);
      touch(u < 64 ? wdr : wdi, (u < 64 ? ul : ul - 3136) * 128, ldsb + JUNK);
      touch(win, (ul / 14) * 3584 + (ul % 14) * 128, ldsb + JUNK);
      const char* pw = (const char*)am + (size_t)(mi * 128 + lw * 32) * 4096;
#pragma unroll 1
      for (int j = 0; j < 4; ++j) {
        touch(pw + (j >> 1) * 512 + (j & 1) * (16 * 4096), pwo, ldsb + JUNK);
        if (j == 3) touch(wout, ul * 128, ldsb + JUNK);
        WAITV(1);
      }
      WAITV(0);
    } else {
      WAITV(12);
      BAR();
#pragma unroll 1
      for (int s = 4; s < 7; ++s) {
        stage1(s);
        WAITV(12);
        BAR();
      }
      const u16* dm = zg < 7 ? wdr : wdi;
      const char* dg_ = (const char*)dm + (size_t)((zg < 7 ? zg : zg - 7) * 64 + lw * 16) * 896;
      const unsigned do_ = l3 * 896 + ((l7 ^ (l3 >> 1)) << 4);
      auto dchunk = [&](int c) {
        const unsigned st = (o7 >> (4 * c)) & 7;
#pragma unroll
        for (int i = 0; i < 2; ++i) glds(dg_ + st * 128 + i * (8 * 896), do_ ^ (i << 6), ldsb + c * 8192 + lw * 2048 + i * 1024);
      };
      auto wchunk = [&](int c) {
        const unsigned st = (o7 >> (4 * c)) & 7;
#pragma unroll
        for (int i = 0; i < 2; ++i)
          glds(wg + st * (64 * 3584) + i * (8 * 3584), wo ^ (i << 6), ldsb + (c ? W1 + c * 8192 : W1C0) + lw * 2048 + i * 1024);
      };
      dchunk(0); dchunk(1); dchunk(2);
      WAITV(12);
      BAR();
      dchunk(3); dchunk(4); dchunk(5);
      WAITV(12);
      BAR();
      dchunk(6);
#pragma unroll 1
      for (int c = 0; c < 7; ++c) wchunk(c);
      WAITV(14);
      BAR();
      if (G && lw < 2) touch((const char*)am + (size_t)(mi * 128 + ia * 16) * 4096 + lw * 512, pwo, ldsb + JUNK);
      __builtin_amdgcn_s_sleep(14);
      const __amdgpu_buffer_rsrc_t rx = __builtin_amdgcn_make_buffer_rsrc(ws, 0, WS_USED, 0x00020000);
      const u64 tag40 = tag << 24;
      u32x4 gr[7];
#pragma unroll 1
      for (unsigned spins = 0; spins < SPIN_CAP; ++spins) {
        unsigned bh = 0, bl = 0;
        asm volatile("" ::: "memory");
#pragma unroll
        for (int c = 0; c < 7; ++c)
          gr[c] = __builtin_amdgcn_raw_buffer_load_b128(rx, lane * 16 + (c & 3) * 1024, OFF_X + (4 * mi + lw) * 7168 + (c >> 2) * 4096, 16);
#pragma unroll
        for (int c = 0; c < 7; ++c) {
          bh |= (gr[c][1] ^ (unsigned)(tag40 >> 32)) | (gr[c][3] ^ (unsigned)(tag40 >> 32));
          bl |= (gr[c][0] ^ (unsigned)tag40) | (gr[c][2] ^ (unsigned)tag40);
        }
        if (__all((bh | (bl >> 24)) == 0)) break;
      }
      auto both = [](float v) {
        auto q = __builtin_amdgcn_permlane32_swap(__float_as_uint(v), __float_as_uint(v), false, false);
        return __uint_as_float(q[0]) + __uint_as_float(q[1]);
      };
      float sum = 0.f, m2 = 0.f, dev = 0.f;
#pragma unroll
      for (int c = 0; c < 7; ++c) { sum += __uint_as_float(gr[c][0] << 8); m2 += __uint_as_float(gr[c][2] << 8); }
      const float mean = both(sum) * (1.0f / 14.0f);
#pragma unroll
      for (int c = 0; c < 7; ++c) { const float d = __uint_as_float(gr[c][0] << 8) - mean; dev = __builtin_fmaf(d, d, dev); }
      const float rstd = __builtin_amdgcn_rsqf(both(__builtin_fmaf(dev, 64.0f, m2)) * (1.0f / 896.0f) + 1e-5f);
      *(LAS float*)(lds + STAT + lw * 256 + (lane >> 5) * 128 + (8 * ((lane & 15) >> 2) + 4 * ((lane >> 4) & 1) + (lane & 3)) * 4) =
          lane < 32 ? rstd : -mean * rstd;
      WAITV(0);
      WAITL0();
      BAR();
    }
    const bool isA = lw < 2;
    unsigned off[8];
#pragma unroll
    for (int i = 0; i < 8; ++i) {
      off[i] = (isA ? (8 * i + l3) * 4096 + ((l7 ^ (l4 | ((i & 3) << 1))) << 4) : i * 1024 + lane * 16) - (i & 3) * 1024;
      asm volatile("" : "+v"(off[i]));
    }
    const char* base2 = isA ? (const char*)am + (size_t)(mi * 128 + lw * 64) * 4096 : ws + OFF_BT + (size_t)p * 64 * 7 * 1024 + (lw - 2) * 7168;
    const unsigned step2 = isA ? 128 : STB, l0 = ldsb + (isA ? lw * 8192 : STA + (lw - 2) * 7168);
    const int pj = p & 7, g0 = (7 * pj) >> 2, g1 = (7 * pj + 6) >> 2;
    const int g2 = g1 - g0 == 2 ? g0 + 1 : (pj & 1 ? 14 + (pj >> 2) : g0);
    unsigned f1o = OFF_F1 + ((lane < 16 ? g0 : (lane < 32 ? g1 : g2)) * 16 + (lane & 15)) * 128;
    unsigned f3o = OFF_F3 + (mi * 16 + ((lane & 2) ? (g1 == zg ? g0 : g1) : (g0 == zg ? g1 : g0))) * 128;
    unsigned wo_off = l3 * 896 + (((((l7 >> 1) ^ ((lane >> 4) & 3)) << 1) | (l7 & 1)) << 4);
    asm volatile("" : "+v"(f1o), "+v"(f3o), "+v"(wo_off));
    const int qraise = (!G && lw == 0) ? 1 : -1, qflag = (G && lw == 0) ? 2 : -1, qz = G ? 3 + (ib >> 1) : -1;
    SCHED();
    const int np = cls + (cls < 2 ? 6 : 2), nl = cls + 26;
    int n = 0;
#pragma unroll 1
    for (; n < np; ++n) {
      const int m = n - 1;
      const int st = n == 0 ? cls : ((m & 3) == cls ? m + 4 : m);
      const char* g = base2 + (size_t)st * step2;
      unsigned l = l0 + (n & (RING - 1)) * STG;
      asm volatile("" : "+s"(g), "+s"(l));
      if (n >= 4) {
        WAITV(14);
        BAR();
      }
      if (n == 0) {
#pragma unroll 1
        for (unsigned spins = 0; spins < SPIN_CAP && !spare; ++spins) {
          if (__hip_atomic_load((LAS unsigned*)(lds + CNT), __ATOMIC_RELAXED, __HIP_MEMORY_SCOPE_WORKGROUP) == 4) break;
          __builtin_amdgcn_s_sleep(1);
        }
        if (!isA) {
          if (!spare) __builtin_amdgcn_s_sleep(10);
          wait_flags((GAS u64*)(ws + f1o), tag);
        }
      }
      if (isA) glds_a8(g, off, l, l + 4096); else glds_b7(g, off, l, l + 4096);
      if (n == 3) {
        WAITV(21);
        BAR();
      }
    }
    {
      unsigned ls[4][2];
#pragma unroll
      for (int j = 0; j < 4; ++j) {
        ls[j][0] = l0 + ((np + j) & (RING - 1)) * STG;
        ls[j][1] = ls[j][0] + 4096;
        asm volatile("" : "+s"(ls[j][0]), "+s"(ls[j][1]));
      }
      auto fast = [&](auto AC) {
        constexpr bool A = decltype(AC)::value;
        constexpr unsigned STEP = A ? 128 : STB;
        const char* g = base2 + (size_t)(np - 1) * STEP;
#pragma unroll 1
        for (int q = 0; n < nl; n += 4, ++q) {
          const char* gl = base2 + (size_t)(n + 6) * STEP;
          asm volatile("" : "+s"(gl));
#pragma unroll
          for (int j = 0; j < 4; ++j) {
            if (A) WAITV(16); else WAITV(14);
            BAR();
            if (A) glds_a8(j < 3 ? g : gl, off, ls[j][0], ls[j][1]); else glds_b7(j < 3 ? g : gl, off, ls[j][0], ls[j][1]);
            g += STEP;
            asm volatile("" : "+s"(g));
          }
          if (A && __builtin_expect(q == qraise, 0) && lane == 0)
            __hip_atomic_store((GAS u64*)(ws + OFF_F3 + (mi * 16 + zg) * 128), tag, RLX_AGENT);
          if (__builtin_expect(q == qflag, 0))
            asm volatile("s_mov_b32 m0, %2\n\ts_nop 0\n\tglobal_load_lds_dword %0, %1 sc1" :: "v"(f3o + (lane & 1) * 4), "s"(ws), "s"(ldsb + FLG) : "memory");
          if (__builtin_expect(q == qz, 0)) {
            const unsigned v = *(const LAS unsigned*)(lds + FLG + lane * 4);
            if (!__all(v == (unsigned)(tag >> ((lane & 1) * 32)))) wait_flags((GAS u64*)(ws + f3o), tag);
            const char* zb = ws + OFF_Z1 + (size_t)(2 * (4 * mi + lw) * 28 + ((7 * pj) >> 1)) * 1024;
            const int own = 2 * zg - ((7 * pj) >> 1);
#pragma unroll 1
            for (int i = 0; i < 4; ++i) {
              const int k = (own + 2 + (i & 1)) & 3;
              glds<true>(zb + ((i >> 1) * 28 + k) * 1024, lane * 16, ldsb + SPARE + lw * 8192 + ((i >> 1) * 4 + k) * 1024);
            }
          }
        }
#pragma unroll 1
        for (int k = 0; n < NT; ++n, ++k) {
          unsigned l = l0 + (n & (RING - 1)) * STG, l2 = l + 4096;
          asm volatile("" : "+s"(l), "+s"(l2));
          if (A) WAITV(16); else WAITV(14);
          BAR();
          if (A) glds_a8(g, off, l, l2); else glds_b7(g, off, l, l2);
          g += k == 2 ? 2 * STEP : STEP;
          asm volatile("" : "+s"(g));
        }
      };
      if (isA) fast(std::true_type{}); else fast(std::false_type{});
    }
    const char* wo_base = (const char*)wout + (dg < 7 ? dg : 0) * 128 + (size_t)lw * 56 * 896;
#pragma unroll 1
    for (int c = 0; c < 4; ++c) {
      if (c < 3) WAITV(14); else WAITV(7);
      BAR();
      const char* g = wo_base + (size_t)(((c + rot3) & 3) * 224) * 896;
#pragma unroll
      for (int i = 0; i < 7; ++i) glds(g + i * (8 * 896), wo_off, ldsb + c * STG + lw * 7168 + i * 1024);
    }
  } else {
  const int fi = lane & 15, fq = lane >> 4;
  const unsigned arow = 8 * (fi >> 2) + (fi & 3);
  const unsigned tq = fi >> 2, tp = lane & 3;
  const unsigned aoff0 = (32 * wid + arow) * 128 + ((fq ^ (fi >> 1)) << 4);
  const unsigned aoff1 = aoff0 ^ 64;
  const __amdgpu_buffer_rsrc_t rws = __builtin_amdgcn_make_buffer_rsrc(ws, 0, WS_USED, 0x00020000);
  asm volatile("" : "+s"(tag));
  tag += EPOCH_STEP;
  const int kb = 4 * mi + wid;
  unsigned braw[2] = {0, 0};
  u32x4 z1o[2][2];
  if (threadIdx.x < 3) ((LAS unsigned*)(lds + CNT))[threadIdx.x] = 0;
  if (spare) {
    const int lc = lane < 56 ? lane : 55;
    const int row0 = mi * 128 + (p >> 2) * 64 + wid * 16;
    const u32x4 gv = *(const u32x4*)(gw + lc * 8);
    u32x4 xv[16];
#pragma unroll
    for (int i = 0; i < 16; ++i) xv[i] = *(const u32x4*)(x + (size_t)(row0 + i) * D + lc * 8);
    SCHED();
    WAITL0();
    BAR();
    if (threadIdx.x == 0) __hip_atomic_store((GAS u64*)(ws + OFF_F1 + (zg * 16 + mi) * 128), tag, RLX_AGENT);
    float res = 0.f;
#pragma unroll
    for (int i = 0; i < 16; ++i) {
      float a = 0.f;
#pragma unroll
      for (int j = 0; j < 4; ++j) a += bflo(xv[i][j]) * bflo(gv[j]) + bfhi(xv[i][j]) * bfhi(gv[j]);
      a = lane < 56 ? a : 0.f;
      a = xsum(a);
      a += __builtin_bit_cast(float, __builtin_amdgcn_update_dpp(0, __builtin_bit_cast(int, a), 0x128, 0xf, 0xf, false));
      a += __builtin_bit_cast(float, __builtin_amdgcn_update_dpp(0, __builtin_bit_cast(int, a), 0x124, 0xf, 0xf, false));
      a += __builtin_bit_cast(float, __builtin_amdgcn_update_dpp(0, __builtin_bit_cast(int, a), 0x122, 0xf, 0xf, false));
      a += __builtin_bit_cast(float, __builtin_amdgcn_update_dpp(0, __builtin_bit_cast(int, a), 0x121, 0xf, 0xf, false));
      if (lane == i) res = a;
    }
    if (lane < 16) __hip_atomic_store((GAS float*)(ws + OFF_G) + row0 + lane, 1.0f / (1.0f + __expf(-res)), RLX_AGENT);
#pragma unroll
    for (int h = 0; h < 2; ++h) z1o[h][0] = z1o[h][1] = (u32x4){0, 0, 0, 0};
  } else {
    bf16x8 xf[14][2];
    f32x4 z2[2][4], ac[2][4];
    const unsigned fw = ((tq >> 1) & 1) | ((fq & 1) << 1);
    unsigned bw[4];
#pragma unroll
    for (int n = 0; n < 4; ++n) bw[n] = R1 + 16384 + (8 * fq + tq) * 128 + ((n ^ fw) << 5) + tp * 8;
    const unsigned ax0 = R1 + aoff0, ax1 = R1 + aoff1;
    unsigned bto[4];
#pragma unroll
    for (int n = 0; n < 4; ++n) { bto[n] = bt_pos(4 * zg + n, kb); asm volatile("" : "+s"(bto[n])); }
    unsigned d0 = fi * 128 + ((fq ^ (fi >> 1)) << 4);
    unsigned sw0 = SCR + wid * 4096 + arow * 128 + ((fq ^ fi) << 3);
    unsigned sr0 = SCR + wid * 4096 + (8 * fq + tq) * 128 + ((tp ^ ((fq << 2) | tq)) << 3);
    asm volatile("" : "+v"(d0), "+v"(sw0), "+v"(sr0));
    {
      bf16x8 wf[2][2][4];
      auto rd = [&](int t) {
        const unsigned so = slot1(t) - R1;
#pragma unroll
        for (int k = 0; k < 2; ++k) {
#pragma unroll
          for (int h = 0; h < 2; ++h) xf[2 * t + k][h] = *(const LAS bf16x8*)(lds + (k ? ax1 : ax0) + so + h * 512);
#pragma unroll
          for (int n = 0; n < 4; ++n) wf[t & 1][k][n] = CAT(TRD(lds + bw[n] + so + k * 4096), TRD(lds + bw[n] + so + k * 4096 + 512));
        }
      };
      auto mm = [&](int t) {
#pragma unroll
        for (int k = 0; k < 2; ++k)
#pragma unroll
          for (int n = 0; n < 4; ++n)
#pragma unroll
            for (int h = 0; h < 2; ++h) z2[h][n] = MFMA(wf[t & 1][k][n], xf[2 * t + k][h], z2[h][n]);
      };
#pragma unroll
      for (int h = 0; h < 2; ++h)
#pragma unroll
        for (int n = 0; n < 4; ++n) z2[h][n] = (f32x4){0, 0, 0, 0};
      WAITV(3);
      WAITL0();
      BAR();
      rd(0);
      WAITV(0);
      braw[0] = bsp[mi * 128 + 32 * wid + arow];
      braw[1] = bsp[mi * 128 + 32 * wid + arow + 4];
#pragma unroll
      for (int t = 0; t < 6; ++t) {
        SCHED();
        WAITL0(); BAR();
        rd(t + 1); mm(t);
#pragma unroll
        for (int i = 0; i < 10; ++i) { SGB(0x8, 1); SGB(0x100, 2); }
        SGB(0x8, 6);
      }
      SCHED();
      mm(6);
    }
    {
      float mean_p[2], m2_p[2];
#pragma unroll
      for (int h = 0; h < 2; ++h) {
        f32x4 s4 = {0, 0, 0, 0};
#pragma unroll
        for (int n = 0; n < 4; ++n) { z2[h][n] = gelu4(z2[h][n]); s4 += z2[h][n]; }
        mean_p[h] = xsum((s4[0] + s4[1]) + (s4[2] + s4[3])) * (1.0f / 64.0f);
      }
#pragma unroll
      for (int h = 0; h < 2; ++h) {
        f32x4 q4 = {0, 0, 0, 0};
#pragma unroll
        for (int n = 0; n < 4; ++n) { const f32x4 d = z2[h][n] - mean_p[h]; q4 += d * d; }
        m2_p[h] = xsum((q4[0] + q4[1]) + (q4[2] + q4[3]));
      }
      const float vm = (fq & 1) ? mean_p[1] : mean_p[0], vq = (fq & 1) ? m2_p[1] : m2_p[0];
      const unsigned tl = (unsigned)(tag << 24), th = (unsigned)(tag >> 8);
      asm volatile("" : "+v"(braw[0]), "+v"(braw[1]));
      if (lane < 32)
        st128(rws, (u32x4){tl | ((__float_as_uint(vm) + 0x80u) >> 8), th, tl | ((__float_as_uint(vq) + 0x80u) >> 8), th},
              lane * 16 + OFF_X + (kb * 14 + zg) * 512);
    }
    SCHED();
    BAR();
    f32x4 zt[4][2];
    {
      bf16x8 df[14][4];
      u32x2 tv[4][2];
      auto rdd = [&](int ks) {
#pragma unroll
        for (int n = 0; n < 4; ++n) df[ks][n] = *(const LAS bf16x8*)(lds + ((ks & 1) ? d0 ^ 64 : d0) + (ks >> 1) * 8192 + n * 2048);
      };
      rdd(0); rdd(1);
#pragma unroll
      for (int h = 0; h < 2; ++h)
#pragma unroll
        for (int n = 0; n < 4; ++n) ac[h][n] = (f32x4){0, 0, 0, 0};
      SCHED();
#pragma unroll
      for (int ks = 0; ks < 14; ++ks) {
        if (ks + 2 < 14) rdd(ks + 2);
        if (ks < 4) {
#pragma unroll
          for (int h = 0; h < 2; ++h)
            *(LAS u32x2*)(lds + (sw0 ^ (ks << 5)) + h * 512) = (u32x2){pk_bf16(z2[h][ks][0], z2[h][ks][1]), pk_bf16(z2[h][ks][2], z2[h][ks][3])};
        } else if (ks < 8) {
#pragma unroll
          for (int e = 0; e < 2; ++e) {
            const bf16x4 v4 = TRD(lds + (sr0 ^ ((ks - 4) << 5)) + e * 512);
            tv[ks - 4][e] = *(const u32x2*)&v4;
          }
        } else if (ks < 12) {
#pragma unroll
          for (int e = 0; e < 2; ++e) {
            const u32x2 v = tv[ks - 8][e];
            zt[ks - 8][e] = (f32x4){bflo(v[0]), bfhi(v[0]), bflo(v[1]), bfhi(v[1])};
          }
        }
#pragma unroll
        for (int n = 0; n < 4; ++n)
#pragma unroll
          for (int h = 0; h < 2; ++h) ac[h][n] = MFMA(xf[ks][h], df[ks][n], ac[h][n]);
        if (ks < 4) {
#pragma unroll
          for (int i = 0; i < 4; ++i) { SGB(0x8, 1); SGB(0x100, 1); }
          SGB(0x8, 1); SGB(0x2, 2); SGB(0x8, 1); SGB(0x2, 2); SGB(0x8, 1); SGB(0x2, 1); SGB(0x200, 2); SGB(0x8, 1);
        } else if (ks < 8) {
          SGB(0x2, 1);
#pragma unroll
          for (int i = 0; i < 6; ++i) { SGB(0x8, 1); SGB(0x100, 1); }
          SGB(0x8, 2);
        } else if (ks < 12) {
#pragma unroll
          for (int i = 0; i < 4; ++i) { SGB(0x8, 1); SGB(0x100, 1); SGB(0x8, 1); SGB(0x2, 2); }
#pragma unroll
          for (int e = 0; e < 2; ++e) asm volatile("" : "+v"(zt[ks - 8][e]));
        }
        SCHED();
      }
#pragma unroll
      for (int n = 0; n < 4; ++n) {
        const u32x4 o = {pk_bf16(ac[0][n][0], ac[0][n][1]), pk_bf16(ac[0][n][2], ac[0][n][3]),
                         pk_bf16(ac[1][n][0], ac[1][n][1]), pk_bf16(ac[1][n][2], ac[1][n][3])};
        st128(rws, o, lane * 16 + bto[n]);
      }
    }
    SCHED();
    BAR();
    bf16x8 wf[14][4];
    auto rdw = [&](int ks) {
#pragma unroll
      for (int n = 0; n < 4; ++n) {
        LAS char* a = lds + (bw[n] - (R1 + 16384 - W1C0)) + (ks < 2 ? 0 : W1 - W1C0) + ks * 4096;
        wf[ks][n] = CAT(TRD(a), TRD(a + 512));
      }
    };
    {
      f32x4 rs[2], mu[2];
#pragma unroll
      for (int e = 0; e < 2; ++e) {
        rs[e] = *(const LAS f32x4*)(lds + STAT + wid * 256 + fq * 32 + e * 16);
        mu[e] = *(const LAS f32x4*)(lds + STAT + wid * 256 + 128 + fq * 32 + e * 16);
      }
      SCHED();
      rdw(0); rdw(1);
      SCHED();
#pragma unroll
      for (int n = 0; n < 4; ++n) {
        const f32x4 v = zt[n][0] * rs[0] + mu[0], w = zt[n][1] * rs[1] + mu[1];
        st128(rws, (u32x4){pk_bf16(v[0], v[1]), pk_bf16(v[2], v[3]), pk_bf16(w[0], w[1]), pk_bf16(w[2], w[3])}, lane * 16 + bto[n] + 8 * 64 * 7 * 1024);
      }
    }
#pragma unroll
    for (int h = 0; h < 2; ++h)
#pragma unroll
      for (int n = 0; n < 4; ++n) ac[h][n] = (f32x4){0, 0, 0, 0};
    SCHED();
#pragma unroll
    for (int ks = 0; ks < 14; ++ks) {
      if (ks + 2 < 14) rdw(ks + 2);
#pragma unroll
      for (int n = 0; n < 4; ++n)
#pragma unroll
        for (int h = 0; h < 2; ++h) ac[h][n] = MFMA(wf[ks][n], xf[ks][h], ac[h][n]);
      if (ks + 2 < 14) {
#pragma unroll
        for (int i = 0; i < 8; ++i) { SGB(0x8, 1); SGB(0x100, 1); }
      }
      SCHED();
      if (ks == 6) { arrive_and_flag(lds, 0, ws, OFF_F1 + (zg * 16 + mi) * 128, tag, lane); SCHED(); }
    }
#pragma unroll
    for (int h = 0; h < 2; ++h)
#pragma unroll
      for (int k = 0; k < 2; ++k) {
        const f32x4 v = gelu4(ac[h][2 * k]), w = gelu4(ac[h][2 * k + 1]);
        z1o[h][k] = (u32x4){pk_bf16(v[0], v[1]), pk_bf16(v[2], v[3]), pk_bf16(w[0], w[1]), pk_bf16(w[2], w[3])};
      }
  }
  f32x4 acc[2][7];
#pragma unroll
  for (int m = 0; m < 2; ++m) {
    const float bv = G ? bflo(braw[m]) : 0.f;
#pragma unroll
    for (int n = 0; n < 7; ++n) { acc[m][n] = (f32x4){bv, bv, bv, bv}; asm volatile("" : "+v"(acc[m][n])); }
  }
  u32x4 zq[2][3];
  u32x2 zs[2];
  {
    const unsigned boff = STA + lane * 16;
    bf16x8 fa[2][2][2], fb[2][2][7];
    unsigned ba[2][2] = {{aoff0, aoff1}, {aoff0 + 2 * STG, aoff1 + 2 * STG}}, bb[2] = {boff, boff + 2 * STG};
    asm volatile("" : "+v"(ba[1][0]), "+v"(ba[1][1]), "+v"(bb[1]));
    auto rd = [&](int set, int slot) {
      const int so = (slot & 1) * STG;
#pragma unroll
      for (int k = 0; k < 2; ++k) {
#pragma unroll
        for (int m = 0; m < 2; ++m) fa[set][k][m] = *(const LAS bf16x8*)(lds + ba[slot >> 1][k] + (so + m * 512));
#pragma unroll
        for (int n = 0; n < 7; ++n) fb[set][k][n] = *(const LAS bf16x8*)(lds + bb[slot >> 1] + (so + k * 7168 + n * 1024));
      }
    };
    auto mm = [&](int set) {
#pragma unroll
      for (int k = 0; k < 2; ++k)
#pragma unroll
        for (int n = 0; n < 7; ++n)
#pragma unroll
          for (int m = 0; m < 2; ++m) acc[m][n] = MFMA(fb[set][k][n], fa[set][k][m], acc[m][n]);
    };
    auto sched = [&]() {
#pragma unroll
      for (int i = 0; i < 18; ++i) { SGB(0x8, 1); SGB(0x100, 1); }
      SGB(0x8, 10);
      SCHED();
    };
    unsigned zw0 = SPARE + wid * 8192 + lane * 16 + (G ? (2 * zg - ((7 * (p - 8)) >> 1)) * 1024 : 0);
    unsigned zw1 = G ? zw0 : zw0 + 2048;
    unsigned zso = (G || spare) ? 0x7ff00000u : lane * 16 + OFF_Z1 + (2 * kb * 28 + 2 * zg) * 1024;
    asm volatile("" : "+v"(zw0), "+v"(zw1), "+v"(zso));
    WAITL0();
    BAR();
    rd(0, 0);
#pragma unroll
    for (int j = 0; j < 4; ++j) st128(rws, z1o[j >> 1][j & 1], zso + ((j >> 1) * 28 + (j & 1)) * 1024);
    if (!G) {
#pragma unroll
      for (int j = 0; j < 4; ++j) z1o[j >> 1][j & 1] = (u32x4){0x3f803f80u, 0x3f803f80u, 0x3f803f80u, 0x3f803f80u};
    }
    SCHED();
#pragma unroll 1
    for (int t = 0; t < 7; ++t) {
      WAITL0(); BAR(); rd(1, 1); mm(0); sched();
      WAITL0(); BAR(); rd(0, 2); mm(1); sched();
      WAITL0(); BAR(); rd(1, 3); mm(0); sched();
      WAITL0(); BAR(); rd(0, 0); mm(1); sched();
      WAITV(0);
    }
    WAITL0(); BAR(); rd(1, 1); mm(0);
#pragma unroll
    for (int j = 0; j < 4; ++j) *(LAS u32x4*)(lds + zw0 + ((j >> 1) * 4 + (j & 1)) * 1024) = z1o[j >> 1][j & 1];
#pragma unroll
    for (int j = 0; j < 4; ++j) *(LAS u32x4*)(lds + zw1 + ((j >> 1) * 4 + (j & 1)) * 1024) = z1o[j >> 1][j & 1];
#pragma unroll
    for (int i = 0; i < 18; ++i) { SGB(0x8, 1); SGB(0x100, 1); }
#pragma unroll
    for (int i = 0; i < 8; ++i) { SGB(0x8, 1); SGB(0x200, 1); }
    SGB(0x8, 2);
    SCHED();
    WAITL0(); BAR(); rd(0, 2); mm(1); sched();
    WAITL0(); BAR(); rd(1, 3); mm(0); sched();
    WAITL0(); BAR();
    {
      const int odd = p & 1;
      LAS char* zl = lds + SPARE + wid * 8192 + lane * 16;
#pragma unroll
      for (int m = 0; m < 2; ++m) {
#pragma unroll
        for (int k = 0; k < 3; ++k) zq[m][k] = *(const LAS u32x4*)(zl + odd * 1024 + (m * 4 + k) * 1024);
        zs[m] = *(const LAS u32x2*)(zl + (odd ? 8 : 3072) + m * 4096);
      }
    }
    auto fin = [&](f32x4 v, unsigned z0, unsigned z1) {
      return (u32x2){pk_bf16(v[0] * bflo(z0), v[1] * bfhi(z0)), pk_bf16(v[2] * bflo(z1), v[3] * bfhi(z1))};
    };
    const unsigned o2 = lane * 16 + OFF_O2 + (2 * kb * 48 + p * 3) * 1024;
#pragma unroll
    for (int g = 0; g < 5; ++g) {
      if (g < 4) {
#pragma unroll
        for (int k = 0; k < 2; ++k)
#pragma unroll
          for (int n = 2 * g; n < 2 * g + 2 && n < 7; ++n)
#pragma unroll
            for (int m = 0; m < 2; ++m) acc[m][n] = MFMA(fb[1][k][n], fa[1][k][m], acc[m][n]);
      }
      if (g == 4) {
        const u32x2 lo = fin(acc[0][6], zs[0][0], zs[0][1]), hi = fin(acc[1][6], zs[1][0], zs[1][1]);
        st128(rws, (u32x4){lo[0], lo[1], hi[0], hi[1]}, lane * 16 + OFF_O1 + (kb * 16 + p) * 1024);
      } else if (g > 0) {
        const int k = g - 1;
#pragma unroll
        for (int m = 0; m < 2; ++m) {
          const u32x2 lo = fin(acc[m][2 * k], zq[m][k][0], zq[m][k][1]), hi = fin(acc[m][2 * k + 1], zq[m][k][2], zq[m][k][3]);
          st128(rws, (u32x4){lo[0], lo[1], hi[0], hi[1]}, o2 + (m * 48 + k) * 1024);
        }
#pragma unroll
        for (int i = 0; i < (g < 3 ? 8 : 4); ++i) { SGB(0x8, 1); SGB(0x2, 4); }
      }
      SCHED();
    }
  }
  }
  {
    int ln = threadIdx.x;
    asm volatile("" : "+v"(ln));
    ln &= 63;
    const int fi = ln & 15, fq = ln >> 4, kh = 1 - (wid >> 2);
    const unsigned tq = fi >> 2, tp = ln & 3;
    const int mt = 4 * rg + (wid & 3);
    const __amdgpu_buffer_rsrc_t rws = __builtin_amdgcn_make_buffer_rsrc(ws, 0, WS_USED, 0x00020000);
    unsigned wb[4];
#pragma unroll
    for (int n = 0; n < 4; ++n) wb[n] = kh * 2 * STG + (4 * fq + tq) * 128 + ((n ^ (((4 * fq + tq) >> 1) & 3)) << 5) + tp * 8;
    bf16x8 wf[14][4];
    auto rdw = [&](int i) {
#pragma unroll
      for (int n = 0; n < 4; ++n) {
        LAS char* a = lds + wb[n] + ((i / 7) * STG + (i % 7) * 4096);
        wf[i][n] = CAT(TRD(a), TRD(a + 2048));
      }
    };
    constexpr int PRE = 11, PRE1 = 3, HAH = 5;
    unsigned h2[2], h1[2];
#pragma unroll
    for (int c = 0; c < 2; ++c) {
      const int pp = 8 + 2 * ((2 * kh + c + rot3) & 3);
      h2[c] = OFF_O2 + (mt * 48 + pp * 3) * 1024;
      h1[c] = OFF_O1 + ((mt >> 1) * 16 + pp) * 1024 + (mt & 1) * 8;
      asm volatile("" : "+s"(h2[c]), "+s"(h1[c]));
    }
    const int t = 32 * (mt >> 1) + 8 * tq + (fi & 3) + 4 * (mt & 1);
    const int P = 2 * dg + kh, q = P / 7, k7 = P % 7;
    const bool split = k7 == 3;
    unsigned yo = split ? OFF_O1 + ((mt >> 1) * 16 + 2 * q) * 1024 : OFF_O2 + (mt * 48 + q * 6 + (k7 < 3 ? k7 : k7 - 1)) * 1024;
    unsigned yo2 = yo + (split ? 4096 : 12288), go = OFF_G + t * 4, l16 = ln * 16;
    unsigned oo = (t * D + 64 * dg + 32 * kh + 4 * fq) * 2;
    asm volatile("" : "+s"(yo), "+s"(yo2));
    asm volatile("" : "+v"(go), "+v"(l16), "+v"(oo));
    f32x4 oc[4];
#pragma unroll
    for (int n = 0; n < 4; ++n) { oc[n] = (f32x4){0, 0, 0, 0}; asm volatile("" : "+v"(oc[n])); }
#pragma unroll
    for (int n = 0; n < 4; ++n) asm volatile("" : "+v"(wb[n]));
    SCHED();
    if (wid < 4) BAR();
#pragma unroll
    for (int i = 0; i < PRE1; ++i) rdw(i);
    SCHED();
    if (wid >= 4) {
      WAITV(0);
      BAR();
    } else {
      arrive_and_flag(lds, 2, ws, OFF_F2 + (mi * 16 + p) * 128, tag, ln);
    }
    if (r >= 28) return;
#pragma unroll
    for (int i = PRE1; i < PRE; ++i) rdw(i);
    SCHED();
    GAS u64* f2 = (GAS u64*)(ws + OFF_F2 + ((rg >> 1) * 16 + fi) * 128);
    if (wid == 4) {
#pragma unroll 1
      for (unsigned spins = 0; spins < SPIN_CAP; ++spins) {
        if (__hip_atomic_load((LAS unsigned*)(lds + CNT) + 2, __ATOMIC_RELAXED, __HIP_MEMORY_SCOPE_WORKGROUP) == 4) break;
        __builtin_amdgcn_s_sleep(1);
      }
      wait_flags(f2, tag);
      if (b == 0 && ln == 0) __hip_atomic_store((GAS u64*)(ws + OFF_E), tag, RLX_AGENT);
    }
    BAR();
    u32x4 hf[14];
    auto ldh = [&](int i) {
      const int c = i / 7, k = i % 7;
      if (k == 3) {
        const u32x2 lo = __builtin_amdgcn_raw_buffer_load_b64(rws, l16, h1[c], 16);
        const u32x2 hi = __builtin_amdgcn_raw_buffer_load_b64(rws, l16 + 1024, h1[c], 16);
        hf[i] = (u32x4){lo[0], lo[1], hi[0], hi[1]};
      } else {
        hf[i] = __builtin_amdgcn_raw_buffer_load_b128(rws, l16 + (k < 3 ? k : k - 4) * 1024, h2[c] + (k < 3 ? 0 : 3072), 16);
      }
    };
#pragma unroll
    for (int i = 0; i < HAH; ++i) ldh(i);
    u32x4 ya, yb, yc, yd2;
    float g;
    SCHED();
#pragma unroll
    for (int i = 0; i < 14; ++i) {
      if (i + HAH < 14) ldh(i + HAH);
      if (i + HAH == 14) {
        ya = __builtin_amdgcn_raw_buffer_load_b128(rws, l16, yo, 16);
        yb = __builtin_amdgcn_raw_buffer_load_b128(rws, l16, yo2, 16);
        g = __uint_as_float(__builtin_amdgcn_raw_buffer_load_b32(rws, go, 0, 16));
        yc = yd2 = (u32x4){0, 0, 0, 0};
        if (split) {
          yc = __builtin_amdgcn_raw_buffer_load_b128(rws, l16 + 1024, yo, 16);
          yd2 = __builtin_amdgcn_raw_buffer_load_b128(rws, l16 + 1024, yo2, 16);
        }
      }
      SCHED();
#pragma unroll
      for (int n = 0; n < 4; ++n) oc[n] = MFMA(wf[i][n], *(const bf16x8*)&hf[i], oc[n]);
      SCHED();
      if (i + PRE < 14) rdw(i + PRE);
      SCHED();
    }
    LAS char* ex = lds + SPARE + ln * 16;
    *(LAS f32x4*)(ex + wid * 2048) = kh ? oc[0] : oc[2];
    *(LAS f32x4*)(ex + wid * 2048 + 1024) = kh ? oc[1] : oc[3];
    WAITL0();
    BAR();
    const float g1 = 1.0f - g;
    u32x4 yr = ya, yi = yb;
    if (split) {
      const bool up = mt & 1;
      yr = up ? (u32x4){ya[2], ya[3], yc[2], yc[3]} : (u32x4){ya[0], ya[1], yc[0], yc[1]};
      yi = up ? (u32x4){yb[2], yb[3], yd2[2], yd2[3]} : (u32x4){yb[0], yb[1], yd2[0], yd2[1]};
    }
    u32x2 ov[2];
#pragma unroll
    for (int n = 0; n < 2; ++n) {
      const f32x4 o = *(const LAS f32x4*)(ex + (wid ^ 4) * 2048 + n * 1024) + (kh ? oc[2 + n] : oc[n]);
      const f32x4 y = {bflo(yr[2 * n]) - bflo(yi[2 * n]), bfhi(yr[2 * n]) - bfhi(yi[2 * n]),
                       bflo(yr[2 * n + 1]) - bflo(yi[2 * n + 1]), bfhi(yr[2 * n + 1]) - bfhi(yi[2 * n + 1])};
      const f32x4 v = y * g + o * g1;
      ov[n] = (u32x2){pk_bf16(v[0], v[1]), pk_bf16(v[2], v[3])};
    }
#pragma unroll
    for (int n = 0; n < 2; ++n) *(u32x2*)((char*)out + oo + 32 * n) = ov[n];
  }
}

extern "C" void kernel_launch(void* const* d_in, const int* in_sizes, int n_in, void* d_out, int out_size, void* d_ws,
                              size_t ws_size, hipStream_t stream) {
  fused<<<256, 512, 0, stream>>>((const u16*)d_in[0], (const u16*)d_in[1], (const u16*)d_in[2], (const u16*)d_in[3],
                                 (const u16*)d_in[4], (const u16*)d_in[6], (const u16*)d_in[7], (const u16*)d_in[8],
                                 (const u16*)d_in[9], (const u16*)d_in[10], (char*)d_ws, (u16*)d_out);
}
```
